```python
import torch

def _softmax(x, axis=-1):
    return torch.softmax(x, dim=axis)
def _gelu(x):
    return torch.nn.functional.gelu(x, approximate="tanh")
def _silu(x):
    return torch.nn.functional.silu(x)
def _sigmoid(x):
    return torch.sigmoid(x)
def _layernorm(x, axis=-1, eps=1e-5):
    m = x.mean(dim=axis, keepdim=True)
    v = x.var(dim=axis, keepdim=True, unbiased=False)
    return (x - m) / torch.sqrt(v + eps)
def _scatter_add(tgt, idx, src, axis=0):
    return tgt.clone().index_add_(axis, idx, src)
def _segment_sum(src, seg, n):
    out = torch.zeros((n,) + src.shape[1:], dtype=src.dtype)
    return out.index_add_(0, seg, src)
def _pad(x, pw):
    flat = tuple(v for pair in reversed(pw) for v in pair)
    return torch.nn.functional.pad(x, flat)

kernel_name = 'parallel_sum__sliding_block_cached__glu_gated_reglu__decode'

def setup_inputs():

    _w = (lambda a: (lambda t: t.to(torch.bfloat16) if t.is_floating_point() else t)(torch.as_tensor(a)))
    def _A_setup():
        B, Sq, D = 1, 1, 576
        Hq, Hkv, Sk, Dh = 32, 16, 32768, 128
        assert Hq % Hkv == 0, (Hq, Hkv)
        return {
            "x": _w((lambda *s: torch.randn(*s, dtype=torch.bfloat16) * 0.05)(B, Sq, D)),
            "k_cache": _w((lambda *s: torch.randn(*s, dtype=torch.bfloat16) * 1.6)(B, Hkv, Sk, Dh)),
            "v_cache": _w((lambda *s: torch.randn(*s, dtype=torch.bfloat16) * 0.8)(B, Hkv, Sk, Dh)),
            "wq": _w((lambda *s: torch.randn(*s, dtype=torch.bfloat16) * 1.6)(D, Hq * Dh) * (D ** -0.5)),
            "wo": _w((lambda *s: torch.randn(*s, dtype=torch.bfloat16) * 0.8)(Hq * Dh, D) * ((Hq * Dh) ** -0.5)),
        }
    _A_in = _A_setup()

    def _B_setup():
        B, S, D, F = 1, 1, 576, 4608
        return {
            "x": _w((lambda *s: torch.randn(*s, dtype=torch.bfloat16) * 0.05)(B, S, D)),
            "W_gate": _w((lambda *s: torch.randn(*s, dtype=torch.bfloat16) * 0.4)(D, F) * (2.0 / D) ** 0.5),
            "W_up": _w((lambda *s: torch.randn(*s, dtype=torch.bfloat16) * 0.4)(D, F) * (2.0 / D) ** 0.5),
            "W_down": _w((lambda *s: torch.randn(*s, dtype=torch.bfloat16) * 0.4)(F, D) * (1.0 / F) ** 0.5),
        }
    _B_in = _B_setup()

    return {
        "A_x": _A_in["x"],
        "A_k_cache": _A_in["k_cache"],
        "A_v_cache": _A_in["v_cache"],
        "A_wq": _A_in["wq"],
        "A_wo": _A_in["wo"],
        "B_x": _B_in["x"],
        "B_W_gate": _B_in["W_gate"],
        "B_W_up": _B_in["W_up"],
        "B_W_down": _B_in["W_down"],
    }

def reference(A_x, A_k_cache, A_v_cache, A_wq, A_wo, B_x, B_W_gate, B_W_up, B_W_down):
    def _A_ref(x, k_cache, v_cache, wq, wo):
        B, Sq, D = x.shape
        Hq, Dh = 32, 128
        Hkv, Sk = k_cache.shape[1], k_cache.shape[2]
        W = min(4096, max(256, Sk // 4))
        rep = Hq // Hkv
        q = (lambda x, *d: x.permute(*d))((x @ wq).reshape(B, Sq, Hq, Dh), 0, 2, 1, 3)
        k = (lambda x, n, axis: torch.repeat_interleave(x, n, dim=axis))(k_cache[:, :, Sk - W:, :], rep, axis=1)
        v = (lambda x, n, axis: torch.repeat_interleave(x, n, dim=axis))(v_cache[:, :, Sk - W:, :], rep, axis=1)
        scores = torch.einsum("bhqd,bhkd->bhqk", q, k) * (Dh ** -0.5)
        p = _softmax(scores, axis=-1)
        o = torch.einsum("bhqk,bhkd->bhqd", p, v)
        return (lambda x, *d: x.permute(*d))(o, 0, 2, 1, 3).reshape(B, Sq, Hq * Dh) @ wo

    def _B_ref(x, W_gate, W_up, W_down):
        gate = torch.einsum("bsd,df->bsf", x, W_gate)
        up = torch.einsum("bsd,df->bsf", x, W_up)
        gate = gate * (lambda x, dt: x.to(dt))(gate > 0, gate.dtype)
        h = gate * up
        return torch.einsum("bsf,fd->bsd", h, W_down)

    x = A_x
    xn = _layernorm(x, axis=-1)
    return x + _A_ref(xn, A_k_cache, A_v_cache, A_wq, A_wo) + _B_ref(xn, B_W_gate, B_W_up, B_W_down)
```

```cpp
#include <hip/hip_runtime.h>
#include <hip/hip_bf16.h>
#include <stdint.h>

typedef unsigned v4u __attribute__((ext_vector_type(4)));
typedef unsigned short u16;

constexpr int D = 576, HQ = 32, HKV = 16, SK = 32768, DH = 128, W = 4096, F = 4608;
constexpr int NQ = HQ * DH;

__device__ __forceinline__ float bf2f(unsigned b) { return __uint_as_float(b << 16); }
__device__ __forceinline__ float rbf(float f) {
  unsigned u = __float_as_uint(f);
  u += 0x7FFFu + ((u >> 16) & 1u);
  return __uint_as_float(u & 0xFFFF0000u);
}
__device__ __forceinline__ u16 f2bf(float f) {
  unsigned u = __float_as_uint(f);
  u += 0x7FFFu + ((u >> 16) & 1u);
  return (u16)(u >> 16);
}
__device__ __forceinline__ float lo(unsigned w) { return __uint_as_float(w << 16); }
__device__ __forceinline__ float hi(unsigned w) { return __uint_as_float(w & 0xFFFF0000u); }

__device__ void layernorm(const u16* x, float* xn, float* red) {
  const int t = threadIdx.x;
  float a = bf2f(x[t]);
  float b = (t < 64) ? bf2f(x[512 + t]) : 0.f;
  float s = a + b;
  for (int m = 32; m >= 1; m >>= 1) s += __shfl_xor(s, m);
  if ((t & 63) == 0) red[t >> 6] = s;
  __syncthreads();
  float tot = 0.f;
  for (int i = 0; i < 8; ++i) tot += red[i];
  const float mean = tot * (1.0f / D);
  __syncthreads();
  float da = a - mean, db = (t < 64) ? b - mean : 0.f;
  float v = da * da + db * db;
  for (int m = 32; m >= 1; m >>= 1) v += __shfl_xor(v, m);
  if ((t & 63) == 0) red[t >> 6] = v;
  __syncthreads();
  float vt = 0.f;
  for (int i = 0; i < 8; ++i) vt += red[i];
  const float var = rbf(vt * (1.0f / D));
  const float mb = rbf(mean);
  const float sd = rbf(sqrtf(rbf(var + 1e-5f)));
  xn[t] = rbf(rbf(a - mb) / sd);
  if (t < 64) xn[512 + t] = rbf(rbf(b - mb) / sd);
  __syncthreads();
}

__global__ void __launch_bounds__(512) k_proj(const u16* x, const u16* wq, const u16* wg, const u16* wu, u16* q, u16* h) {
  __shared__ float xn[D];
  __shared__ float red[16];
  __shared__ float part[2][64][65];
  layernorm(x, xn, red);
  const int t = threadIdx.x, ch = t & 7, rg = t >> 3;
  const int u = blockIdx.x;
  const bool isq = u < 64;
  const int N = isq ? NQ : F;
  const int col0 = (isq ? u : u - 64) * 64;
  const int nm = isq ? 1 : 2;
  for (int m = 0; m < nm; ++m) {
    const u16* Wp = isq ? wq : (m == 0 ? wg : wu);
    float acc[8] = {0, 0, 0, 0, 0, 0, 0, 0};
    for (int i = 0; i < 9; ++i) {
      const int r = rg + 64 * i;
      const v4u w = *(const v4u*)(Wp + (size_t)r * N + col0 + 8 * ch);
      const float xv = xn[r];
      for (int k = 0; k < 4; ++k) {
        acc[2 * k] = fmaf(xv, lo(w[k]), acc[2 * k]);
        acc[2 * k + 1] = fmaf(xv, hi(w[k]), acc[2 * k + 1]);
      }
    }
    for (int k = 0; k < 8; ++k) part[m][rg][8 * ch + k] = acc[k];
  }
  __syncthreads();
  if (t < 64) {
    float s0 = 0.f, s1 = 0.f;
    for (int r = 0; r < 64; ++r) { s0 += part[0][r][t]; if (!isq) s1 += part[1][r][t]; }
    if (isq) q[col0 + t] = f2bf(s0);
    else {
      float g = rbf(s0), up = rbf(s1);
      g = g > 0.f ? g : 0.f;
      h[col0 + t] = f2bf(g * up);
    }
  }
}

__global__ void __launch_bounds__(512) k_attn(const u16* q, const u16* kc, const u16* vc, float* pml, float* po) {
  __shared__ float sc[2][256];
  __shared__ float mrg[8][2][132];
  const int s = blockIdx.x, hh = blockIdx.y;
  const int t = threadIdx.x, lane = t & 63, w = t >> 6, r = lane >> 4, c = lane & 15;
  const size_t base = ((size_t)hh * SK + (SK - W) + 256 * s + 32 * w) * DH;
  v4u qv[2];
  for (int g = 0; g < 2; ++g) qv[g] = *(const v4u*)(q + (2 * hh + g) * DH + 8 * c);
  for (int i = 0; i < 8; ++i) {
    const v4u kv = *(const v4u*)(kc + base + (size_t)(4 * i + r) * DH + 8 * c);
    for (int g = 0; g < 2; ++g) {
      float d = 0.f;
      for (int k = 0; k < 4; ++k) { d = fmaf(lo(kv[k]), lo(qv[g][k]), d); d = fmaf(hi(kv[k]), hi(qv[g][k]), d); }
      for (int m = 8; m >= 1; m >>= 1) d += __shfl_xor(d, m);
      if (c == 0) sc[g][32 * w + 4 * i + r] = rbf(rbf(d) * 0.08838834764831845f);
    }
  }
  __syncthreads();
  float mx[2], l[2];
  for (int g = 0; g < 2; ++g) {
    float m = -INFINITY;
    for (int j = 0; j < 256; ++j) m = fmaxf(m, sc[g][j]);
    mx[g] = m;
  }
  float acc[2][8];
  for (int g = 0; g < 2; ++g) { l[g] = 0.f; for (int k = 0; k < 8; ++k) acc[g][k] = 0.f; }
  for (int i = 0; i < 8; ++i) {
    const v4u vv = *(const v4u*)(vc + base + (size_t)(4 * i + r) * DH + 8 * c);
    for (int g = 0; g < 2; ++g) {
      const float p = __expf(sc[g][32 * w + 4 * i + r] - mx[g]);
      l[g] += p;
      for (int k = 0; k < 4; ++k) { acc[g][2 * k] = fmaf(p, lo(vv[k]), acc[g][2 * k]); acc[g][2 * k + 1] = fmaf(p, hi(vv[k]), acc[g][2 * k + 1]); }
    }
  }
  for (int g = 0; g < 2; ++g) {
    for (int k = 0; k < 8; ++k) { acc[g][k] += __shfl_xor(acc[g][k], 16); acc[g][k] += __shfl_xor(acc[g][k], 32); }
    l[g] += __shfl_xor(l[g], 16); l[g] += __shfl_xor(l[g], 32);
    if (r == 0) { for (int k = 0; k < 8; ++k) mrg[w][g][8 * c + k] = acc[g][k]; if (c == 0) mrg[w][g][128] = l[g]; }
  }
  __syncthreads();
  if (t < 256) {
    const int g = t >> 7, d = t & 127;
    float o = 0.f, L = 0.f;
    for (int ww = 0; ww < 8; ++ww) { o += mrg[ww][g][d]; L += mrg[ww][g][128]; }
    const int head = 2 * hh + g;
    po[(size_t)(head * 16 + s) * DH + d] = o;
    if (d == 0) { pml[(head * 16 + s) * 2] = mx[g]; pml[(head * 16 + s) * 2 + 1] = L; }
  }
}

__global__ void __launch_bounds__(576) k_out(const float* pml, const float* po, const u16* h, const u16* wo, const u16* wd, float* pa, float* pb) {
  __shared__ float o[16];
  __shared__ float hv[18];
  __shared__ float part[8][576];
  const int c = blockIdx.x, t = threadIdx.x;
  if (t < 16) {
    const int head = c >> 3, d = (c & 7) * 16 + t;
    float M = -INFINITY;
    for (int s = 0; s < 16; ++s) M = fmaxf(M, pml[(head * 16 + s) * 2]);
    float L = 0.f, a = 0.f;
    for (int s = 0; s < 16; ++s) {
      const float wgt = __expf(pml[(head * 16 + s) * 2] - M);
      L = fmaf(wgt, pml[(head * 16 + s) * 2 + 1], L);
      a = fmaf(wgt, po[(size_t)(head * 16 + s) * DH + d], a);
    }
    o[t] = rbf(a / L);
  }
  if (t >= 64 && t < 82) hv[t - 64] = bf2f(h[18 * c + t - 64]);
  __syncthreads();
  const int ch = t % 72, rg = t / 72;
  for (int m = 0; m < 2; ++m) {
    float acc[8] = {0, 0, 0, 0, 0, 0, 0, 0};
    const int nr = m == 0 ? 16 : 18;
    for (int r = rg; r < nr; r += 8) {
      const v4u w = m == 0 ? *(const v4u*)(wo + (size_t)(16 * c + r) * D + 8 * ch) : *(const v4u*)(wd + (size_t)(18 * c + r) * D + 8 * ch);
      const float xv = m == 0 ? o[r] : hv[r];
      for (int k = 0; k < 4; ++k) { acc[2 * k] = fmaf(xv, lo(w[k]), acc[2 * k]); acc[2 * k + 1] = fmaf(xv, hi(w[k]), acc[2 * k + 1]); }
    }
    for (int k = 0; k < 8; ++k) part[rg][8 * ch + k] = acc[k];
    __syncthreads();
    float sacc = 0.f;
    for (int r = 0; r < 8; ++r) sacc += part[r][t];
    (m == 0 ? pa : pb)[(size_t)c * D + t] = sacc;
    __syncthreads();
  }
}

__global__ void __launch_bounds__(256) k_fin(const u16* x, const float* pa, const float* pb, u16* out) {
  __shared__ float ra[256], rb[256];
  const int n = blockIdx.x, t = threadIdx.x;
  ra[t] = pa[(size_t)t * D + n];
  rb[t] = pb[(size_t)t * D + n];
  __syncthreads();
  for (int s = 128; s >= 1; s >>= 1) { if (t < s) { ra[t] += ra[t + s]; rb[t] += rb[t + s]; } __syncthreads(); }
  if (t == 0) out[n] = f2bf(rbf(bf2f(x[n]) + rbf(ra[0])) + rbf(rb[0]));
}

extern "C" void kernel_launch(void* const* d_in, const int* in_sizes, int n_in, void* d_out, int out_size, void* d_ws, size_t ws_size,
                              hipStream_t stream) {
  const u16* x = (const u16*)d_in[0];
  const u16* kc = (const u16*)d_in[1];
  const u16* vc = (const u16*)d_in[2];
  const u16* wq = (const u16*)d_in[3];
  const u16* wo = (const u16*)d_in[4];
  const u16* wg = (const u16*)d_in[6];
  const u16* wu = (const u16*)d_in[7];
  const u16* wd = (const u16*)d_in[8];
  char* ws = (char*)d_ws;
  u16* q = (u16*)ws;
  u16* h = (u16*)(ws + 16384);
  float* pml = (float*)(ws + 32768);
  float* po = (float*)(ws + 65536);
  float* pa = (float*)(ws + (1 << 20));
  float* pb = (float*)(ws + (2 << 20));
  k_proj<<<136, 512, 0, stream>>>(x, wq, wg, wu, q, h);
  k_attn<<<dim3(16, 16), 512, 0, stream>>>(q, kc, vc, pml, po);
  k_out<<<256, 576, 0, stream>>>(pml, po, h, wo, wd, pa, pb);
  k_fin<<<576, 256, 0, stream>>>(x, pa, pb, (u16*)d_out);
}
```

```cpp
#include <hip/hip_runtime.h>
#include <hip/hip_bf16.h>
#include <stdint.h>

typedef unsigned v4u __attribute__((ext_vector_type(4)));
typedef float f32x4 __attribute__((ext_vector_type(4)));
typedef float f32x2 __attribute__((ext_vector_type(2)));
typedef unsigned short u16;
using bf16x2 = __attribute__((ext_vector_type(2))) __bf16;

constexpr int D = 576, SK = 32768, DH = 128, W = 4096, F = 4608, NQ = 4096;
constexpr int NTHR = 576;
constexpr int WS_E = 0;
constexpr int WS_QG = 4096;
constexpr int WS_AG = WS_QG + 256 * 512;
constexpr int WS_HG = WS_AG + 256 * 2304;
constexpr int WS_FG = WS_HG + 216 * 1024;
constexpr int WS_END = WS_FG + 256 * 4608;
constexpr unsigned SPIN_CAP = 1u << 16;

struct Params {
  const u16 *x, *kc, *vc, *wq, *wo, *wg, *wu, *wd;
  u16* out;
  unsigned char* ws;
};

__device__ __forceinline__ float rbf(float f) {
  unsigned u = __float_as_uint(f);
  u += 0x7FFFu + ((u >> 16) & 1u);
  return __uint_as_float(u & 0xFFFF0000u);
}
__device__ __forceinline__ unsigned f2bf(float f) {
  unsigned u = __float_as_uint(f);
  u += 0x7FFFu + ((u >> 16) & 1u);
  return u >> 16;
}
__device__ __forceinline__ float lo(unsigned w) { return __uint_as_float(w << 16); }
__device__ __forceinline__ float hi(unsigned w) { return __uint_as_float(w & 0xFFFF0000u); }
__device__ __forceinline__ float dot2(unsigned k, unsigned q, float acc) {
  return __builtin_amdgcn_fdot2_f32_bf16(__builtin_bit_cast(bf16x2, k), __builtin_bit_cast(bf16x2, q), acc, false);
}
__device__ __forceinline__ v4u ldw(const u16* p) { return __builtin_nontemporal_load(reinterpret_cast<const v4u*>(p)); }
__device__ __forceinline__ float wave_sum(float v) {
#pragma unroll
  for (int m = 32; m >= 1; m >>= 1) v += __shfl_xor(v, m);
  return v;
}
#define BAR() do { asm volatile("s_waitcnt lgkmcnt(0)" ::: "memory"); __builtin_amdgcn_s_barrier(); asm volatile("" ::: "memory"); } while (0)
#define PIN() __builtin_amdgcn_sched_barrier(0)

template <int H, int M> __device__ __forceinline__ void tr_step(float* v, int lane) {
  const bool up = (lane & M) != 0;
#pragma unroll
  for (int j = 0; j < H; ++j) {
    const float a = v[j], b = v[j + H];
    const float send = up ? a : b, keep = up ? b : a;
    v[j] = keep + __shfl_xor(send, M);
  }
}

__device__ __forceinline__ void fma8(float* acc, float xv, v4u w) {
#pragma unroll
  for (int k = 0; k < 4; ++k) {
    acc[2 * k] = fmaf(xv, lo(w[k]), acc[2 * k]);
    acc[2 * k + 1] = fmaf(xv, hi(w[k]), acc[2 * k + 1]);
  }
}

constexpr int L_XN = 0;
constexpr int L_QP = L_XN + 576;
constexpr int L_QB = L_QP + 576;
constexpr int L_PEX = L_QB + 128;
constexpr int L_MRG = L_PEX + 512;
constexpr int L_GUP = L_MRG + 8 * 264;
constexpr int L_OV = L_GUP + 1024;
constexpr int L_HV = L_OV + 16;
constexpr int L_FIN = L_HV + 32;
constexpr int L_RED = L_FIN + 7 * 576;
constexpr int L_END = L_RED + 160;

__global__ void __launch_bounds__(NTHR, 3) mega_kernel(Params p) {
  __shared__ __attribute__((aligned(16))) float lds[L_END];
  const int tid = threadIdx.x, lane = tid & 63;
  const int w = __builtin_amdgcn_readfirstlane(tid >> 6);
  const int c = blockIdx.x;
  const int h = c >> 4, s = c & 15;
  const bool heavy = (c & 31) < 27;
  const int hu = (c >> 5) * 27 + (c & 31);
  const int li = (c >> 5) * 5 + ((c & 31) - 27);
  const __amdgpu_buffer_rsrc_t rs = __builtin_amdgcn_make_buffer_rsrc((void*)p.ws, 0, WS_END, 0x00020000);

  const v4u xa = *reinterpret_cast<const v4u*>(p.x + 8 * lane);
  v4u xb = {0u, 0u, 0u, 0u};
  if (lane < 8) xb = *reinterpret_cast<const v4u*>(p.x + 512 + 8 * lane);
  const unsigned e0 = *reinterpret_cast<const unsigned*>(p.ws + WS_E);
  const int ch8 = lane & 7, rs8 = lane >> 3;
  const int qrow0 = 144 * (s >> 2) + 16 * w;
  v4u wq0, wq1;
  {
    const u16* b = p.wq + (size_t)(qrow0 + rs8) * NQ + 256 * h + 64 * (s & 3) + 8 * ch8;
    wq0 = ldw(b);
    wq1 = ldw(b + (size_t)8 * NQ);
  }
  const int r4 = lane >> 4, c16 = lane & 15;
  const size_t kvbase = ((size_t)h * SK + (SK - W) + 256 * s + 32 * (w & 7) + r4) * DH + 8 * c16;
  v4u kr[8];
  if (w < 8) {
#pragma unroll
    for (int i = 0; i < 8; ++i) kr[i] = ldw(p.kc + kvbase + (size_t)(4 * i) * DH);
  }
  PIN();
  const unsigned tag0 = e0 + 1u, tag1 = ~tag0;
  {
    float xv[8], xw[8];
#pragma unroll
    for (int k = 0; k < 4; ++k) { xv[2 * k] = lo(xa[k]); xv[2 * k + 1] = hi(xa[k]); xw[2 * k] = lo(xb[k]); xw[2 * k + 1] = hi(xb[k]); }
    float sm = 0.f;
#pragma unroll
    for (int k = 0; k < 8; ++k) sm += xv[k] + xw[k];
    const float mean = wave_sum(sm) * (1.0f / D);
    float vs = 0.f;
#pragma unroll
    for (int k = 0; k < 8; ++k) { const float a = xv[k] - mean; vs = fmaf(a, a, vs); const float b = xw[k] - mean; if (lane < 8) vs = fmaf(b, b, vs); }
    const float var = rbf(wave_sum(vs) * (1.0f / D));
    const float mb = rbf(mean);
    const float sd = rbf(sqrtf(rbf(var + 1e-5f)));
    f32x4 o0, o1;
#pragma unroll
    for (int k = 0; k < 4; ++k) { o0[k] = rbf(rbf(xv[k] - mb) / sd); o1[k] = rbf(rbf(xv[4 + k] - mb) / sd); }
    *reinterpret_cast<f32x4*>(&lds[L_XN + 8 * lane]) = o0;
    *reinterpret_cast<f32x4*>(&lds[L_XN + 8 * lane + 4]) = o1;
    if (lane < 8) {
#pragma unroll
      for (int k = 0; k < 4; ++k) { o0[k] = rbf(rbf(xw[k] - mb) / sd); o1[k] = rbf(rbf(xw[4 + k] - mb) / sd); }
      *reinterpret_cast<f32x4*>(&lds[L_XN + 512 + 8 * lane]) = o0;
      *reinterpret_cast<f32x4*>(&lds[L_XN + 512 + 8 * lane + 4]) = o1;
    }
    asm volatile("s_waitcnt lgkmcnt(0)" ::: "memory");
  }
  {
    float acc[8] = {0, 0, 0, 0, 0, 0, 0, 0};
    const float x0 = lds[L_XN + qrow0 + rs8], x1 = lds[L_XN + qrow0 + 8 + rs8];
    fma8(acc, x0, wq0);
    fma8(acc, x1, wq1);
    tr_step<4, 32>(acc, lane); tr_step<2, 16>(acc, lane); tr_step<1, 8>(acc, lane);
    lds[L_QP + 64 * w + 8 * ch8 + rs8] = acc[0];
  }
  BAR();

  if (w == 8) {
    {
      if (lane < 32) {
        float a = 0.f, b = 0.f;
#pragma unroll
        for (int ww = 0; ww < 9; ++ww) { const f32x2 v = *reinterpret_cast<const f32x2*>(&lds[L_QP + 64 * ww + 2 * lane]); a += v[0]; b += v[1]; }
        const v4u g = {__float_as_uint(a), __float_as_uint(b), tag0, tag1};
        __builtin_amdgcn_raw_buffer_store_b128(g, rs, WS_QG + c * 512 + lane * 16, 0, 16);
      }
    }
    {
      v4u g[8];
      for (unsigned spin = 0;; ++spin) {
        bool ok = true;
#pragma unroll
        for (int k = 0; k < 8; ++k) g[k] = __builtin_amdgcn_raw_buffer_load_b128(rs, WS_QG + (16 * h + (lane >> 5) + 2 * k) * 512 + (lane & 31) * 16, 0, 16);
#pragma unroll
        for (int k = 0; k < 8; ++k) ok &= (g[k][2] == tag0) & (g[k][3] == tag1);
        if (__all(ok) || spin > SPIN_CAP) break;
        asm volatile("" ::: "memory");
      }
      float a[2] = {0.f, 0.f}, b[2] = {0.f, 0.f};
#pragma unroll
      for (int k = 0; k < 8; ++k) { a[k & 1] += __uint_as_float(g[k][0]); b[k & 1] += __uint_as_float(g[k][1]); }
#pragma unroll
      for (int j = 0; j < 2; ++j) {
        const int cg = (lane >> 5) + 2 * j;
        reinterpret_cast<unsigned*>(lds)[L_QB + 32 * cg + (lane & 31)] = f2bf(a[j]) | (f2bf(b[j]) << 16);
      }
    }
    BAR();
    BAR();
    {
#pragma unroll
      for (int g = 0; g < 2; ++g) {
        float mw[8], M = -INFINITY;
#pragma unroll
        for (int ww = 0; ww < 8; ++ww) { mw[ww] = lds[L_MRG + (ww * 2 + g) * 132 + 128]; M = fmaxf(M, mw[ww]); }
        float L = 0.f, a = 0.f, b = 0.f;
#pragma unroll
        for (int ww = 0; ww < 8; ++ww) {
          const float wt = __expf(mw[ww] - M);
          L = fmaf(wt, lds[L_MRG + (ww * 2 + g) * 132 + 129], L);
          const f32x2 v = *reinterpret_cast<const f32x2*>(&lds[L_MRG + (ww * 2 + g) * 132 + 2 * lane]);
          a = fmaf(wt, v[0], a); b = fmaf(wt, v[1], b);
        }
        const int rec = WS_AG + c * 2304 + (g * 8 + (lane >> 3)) * 144;
        const v4u ga = {__float_as_uint(a), __float_as_uint(b), tag0, tag1};
        __builtin_amdgcn_raw_buffer_store_b128(ga, rs, rec + 16 + (lane & 7) * 16, 0, 16);
        if ((lane & 7) == 0) {
          const v4u gm = {__float_as_uint(M), __float_as_uint(L), tag0, tag1};
          __builtin_amdgcn_raw_buffer_store_b128(gm, rs, rec, 0, 16);
        }
      }
    }
    BAR();
    if (heavy) {
      float a = 0.f, b = 0.f;
#pragma unroll
      for (int ww = 0; ww < 8; ++ww) { a += lds[L_GUP + 128 * ww + lane]; b += lds[L_GUP + 128 * ww + 64 + lane]; }
      const v4u g = {__float_as_uint(a), __float_as_uint(b), tag0, tag1};
      __builtin_amdgcn_raw_buffer_store_b128(g, rs, WS_HG + hu * 1024 + lane * 16, 0, 16);
    }
    {
      const int pr = lane >> 2, q4 = lane & 3;
      const int rec = WS_AG + (16 * h + pr) * 2304 + s * 144;
      v4u gm, g0, g1;
      for (unsigned spin = 0;; ++spin) {
        gm = __builtin_amdgcn_raw_buffer_load_b128(rs, rec, 0, 16);
        g0 = __builtin_amdgcn_raw_buffer_load_b128(rs, rec + 16 + 32 * q4, 0, 16);
        g1 = __builtin_amdgcn_raw_buffer_load_b128(rs, rec + 32 + 32 * q4, 0, 16);
        const bool ok = (gm[2] == tag0) & (gm[3] == tag1) & (g0[2] == tag0) & (g0[3] == tag1) & (g1[2] == tag0) & (g1[3] == tag1);
        if (__all(ok) || spin > SPIN_CAP) break;
        asm volatile("" ::: "memory");
      }
      const float m = __uint_as_float(gm[0]);
      float M = m;
#pragma unroll
      for (int k = 4; k <= 32; k <<= 1) M = fmaxf(M, __shfl_xor(M, k));
      const float wt = __expf(m - M);
      float v[5] = {wt * __uint_as_float(gm[1]), wt * __uint_as_float(g0[0]), wt * __uint_as_float(g0[1]), wt * __uint_as_float(g1[0]), wt * __uint_as_float(g1[1])};
#pragma unroll
      for (int j = 0; j < 5; ++j)
#pragma unroll
        for (int k = 4; k <= 32; k <<= 1) v[j] += __shfl_xor(v[j], k);
      if (lane < 4) {
        const float inv = 1.0f / v[0];
        f32x4 o4 = {rbf(v[1] * inv), rbf(v[2] * inv), rbf(v[3] * inv), rbf(v[4] * inv)};
        *reinterpret_cast<f32x4*>(&lds[L_OV + 4 * lane]) = o4;
      }
    }
    {
      const int ks = lane / 18, j = lane - 18 * ks;
      const int f = 18 * c + j;
      const int off = WS_HG + ((ks < 3 ? ks : 0) * 72 + (f >> 6)) * 1024 + (f & 63) * 16;
      v4u g;
      for (unsigned spin = 0;; ++spin) {
        g = __builtin_amdgcn_raw_buffer_load_b128(rs, off, 0, 16);
        const bool ok = (g[2] == tag0) & (g[3] == tag1);
        if (__all(ok) || spin > SPIN_CAP) break;
        asm volatile("" ::: "memory");
      }
      float a = __uint_as_float(g[0]), b = __uint_as_float(g[1]);
      const float a1 = __shfl(a, lane + 18), b1 = __shfl(b, lane + 18), a2 = __shfl(a, lane + 36), b2 = __shfl(b, lane + 36);
      if (lane < 18) {
        float gt = rbf(a + a1 + a2);
        const float up = rbf(b + b1 + b2);
        gt = gt > 0.f ? gt : 0.f;
        lds[L_HV + lane] = rbf(gt * up);
      }
    }
    BAR();
    return;
  }

  v4u vr[8];
#pragma unroll
  for (int i = 0; i < 8; ++i) vr[i] = ldw(p.vc + kvbase + (size_t)(4 * i) * DH);
  PIN();
  BAR();
  v4u gr[3], ur[3];
  const int gurow0 = 192 * (hu / 72) + 24 * w;
  if (heavy) {
    const size_t o = (size_t)(gurow0 + rs8) * F + 64 * (hu % 72) + 8 * ch8;
#pragma unroll
    for (int i = 0; i < 3; ++i) { gr[i] = ldw(p.wg + o + (size_t)(8 * i) * F); ur[i] = ldw(p.wu + o + (size_t)(8 * i) * F); }
  }
  PIN();
  float pv[16];
  {
    v4u qv[2];
#pragma unroll
    for (int g = 0; g < 2; ++g) qv[g] = *reinterpret_cast<const v4u*>(&lds[L_QB + 64 * g + 4 * c16]);
    float x[16];
#pragma unroll
    for (int i = 0; i < 8; ++i)
#pragma unroll
      for (int g = 0; g < 2; ++g) {
        float d = 0.f;
#pragma unroll
        for (int k = 0; k < 4; ++k) d = dot2(kr[i][k], qv[g][k], d);
        x[2 * i + g] = d;
      }
    tr_step<8, 8>(x, lane); tr_step<4, 4>(x, lane); tr_step<2, 2>(x, lane); tr_step<1, 1>(x, lane);
    const float sc = rbf(rbf(x[0]) * 0.08838834764831845f);
    float m = sc;
#pragma unroll
    for (int k = 2; k <= 32; k <<= 1) m = fmaxf(m, __shfl_xor(m, k));
    const float pe = __expf(sc - m);
    float l = pe;
#pragma unroll
    for (int k = 2; k <= 32; k <<= 1) l += __shfl_xor(l, k);
    if (lane < 2) { lds[L_MRG + (w * 2 + lane) * 132 + 128] = m; lds[L_MRG + (w * 2 + lane) * 132 + 129] = l; }
    lds[L_PEX + 64 * w + lane] = pe;
    asm volatile("s_waitcnt lgkmcnt(0)" ::: "memory");
#pragma unroll
    for (int u = 0; u < 4; ++u) {
      const f32x4 t = *reinterpret_cast<const f32x4*>(&lds[L_PEX + 64 * w + 16 * r4 + 4 * u]);
      pv[4 * u] = t[0]; pv[4 * u + 1] = t[1]; pv[4 * u + 2] = t[2]; pv[4 * u + 3] = t[3];
    }
  }
  const int ch72 = tid % 72, rg7 = tid / 72;
  v4u wor[3], wdr[3];
  if (tid < 504) {
#pragma unroll
    for (int i = 0; i < 3; ++i) {
      const int r = rg7 + 7 * i;
      if (r < 18) wdr[i] = ldw(p.wd + (size_t)(18 * c + r) * D + 8 * ch72);
    }
#pragma unroll
    for (int i = 0; i < 3; ++i) {
      const int r = rg7 + 7 * i;
      if (r < 16) wor[i] = ldw(p.wo + (size_t)(16 * c + r) * D + 8 * ch72);
    }
  }
  PIN();
  {
    float acc[16];
#pragma unroll
    for (int k = 0; k < 16; ++k) acc[k] = 0.f;
#pragma unroll
    for (int i = 0; i < 8; ++i) {
      fma8(acc, pv[2 * i], vr[i]);
      fma8(acc + 8, pv[2 * i + 1], vr[i]);
    }
    tr_step<8, 32>(acc, lane); tr_step<4, 16>(acc, lane);
    const f32x4 o4 = {acc[0], acc[1], acc[2], acc[3]};
    *reinterpret_cast<f32x4*>(&lds[L_MRG + (w * 2 + (lane >> 5)) * 132 + 8 * c16 + 4 * ((lane >> 4) & 1)]) = o4;
  }
  BAR();
  if (heavy) {
    float ag[8] = {0, 0, 0, 0, 0, 0, 0, 0}, au[8] = {0, 0, 0, 0, 0, 0, 0, 0};
#pragma unroll
    for (int i = 0; i < 3; ++i) {
      const float xv = lds[L_XN + gurow0 + 8 * i + rs8];
      fma8(ag, xv, gr[i]);
      fma8(au, xv, ur[i]);
    }
    tr_step<4, 32>(ag, lane); tr_step<2, 16>(ag, lane); tr_step<1, 8>(ag, lane);
    tr_step<4, 32>(au, lane); tr_step<2, 16>(au, lane); tr_step<1, 8>(au, lane);
    lds[L_GUP + 128 * w + 8 * ch8 + rs8] = ag[0];
    lds[L_GUP + 128 * w + 64 + 8 * ch8 + rs8] = au[0];
  }
  BAR();
  BAR();
  if (tid < 504) {
    float acc[8] = {0, 0, 0, 0, 0, 0, 0, 0};
#pragma unroll
    for (int i = 0; i < 3; ++i) {
      const int r = rg7 + 7 * i;
      if (r < 18) fma8(acc, lds[L_HV + r], wdr[i]);
      if (r < 16) fma8(acc, lds[L_OV + r], wor[i]);
    }
    const f32x4 a0 = {acc[0], acc[1], acc[2], acc[3]}, a1 = {acc[4], acc[5], acc[6], acc[7]};
    *reinterpret_cast<f32x4*>(&lds[L_FIN + 576 * rg7 + 8 * ch72]) = a0;
    *reinterpret_cast<f32x4*>(&lds[L_FIN + 576 * rg7 + 8 * ch72 + 4]) = a1;
  }
  BAR();
  if (tid < 288) {
    float a = 0.f, b = 0.f;
#pragma unroll
    for (int r = 0; r < 7; ++r) { const f32x2 v = *reinterpret_cast<const f32x2*>(&lds[L_FIN + 576 * r + 2 * tid]); a += v[0]; b += v[1]; }
    const v4u g = {__float_as_uint(a), __float_as_uint(b), tag0, tag1};
    __builtin_amdgcn_raw_buffer_store_b128(g, rs, WS_FG + c * 4608 + tid * 16, 0, 16);
  }
  if (heavy || li >= 36) return;
  {
    const int gi = tid & 7, p0 = tid >> 3;
    v4u g[4];
    for (unsigned spin = 0;; ++spin) {
      bool ok = true;
#pragma unroll
      for (int k = 0; k < 4; ++k) g[k] = __builtin_amdgcn_raw_buffer_load_b128(rs, WS_FG + (p0 + 64 * k) * 4608 + li * 128 + gi * 16, 0, 16);
#pragma unroll
      for (int k = 0; k < 4; ++k) ok &= (g[k][2] == tag0) & (g[k][3] == tag1);
      if (__all(ok) || spin > SPIN_CAP) break;
      asm volatile("" ::: "memory");
    }
    float a = 0.f, b = 0.f;
#pragma unroll
    for (int k = 0; k < 4; ++k) { a += __uint_as_float(g[k][0]); b += __uint_as_float(g[k][1]); }
#pragma unroll
    for (int k = 8; k <= 32; k <<= 1) { a += __shfl_xor(a, k); b += __shfl_xor(b, k); }
    if (lane < 8) { lds[L_RED + 16 * w + 2 * lane] = a; lds[L_RED + 16 * w + 2 * lane + 1] = b; }
    BAR();
    if (tid < 16) {
      float t = 0.f;
#pragma unroll
      for (int ww = 0; ww < 8; ++ww) t += lds[L_RED + 16 * ww + tid];
      const int n = 16 * li + tid;
      const float xv = __uint_as_float((unsigned)p.x[n] << 16);
      p.out[n] = (u16)f2bf(xv + t);
    }
    if (li == 0 && tid == 0) *reinterpret_cast<unsigned*>(p.ws + WS_E) = tag0;
  }
}

extern "C" void kernel_launch(void* const* d_in, const int* in_sizes, int n_in, void* d_out, int out_size, void* d_ws, size_t ws_size,
                              hipStream_t stream) {
  Params p;
  p.x = (const u16*)d_in[0];
  p.kc = (const u16*)d_in[1];
  p.vc = (const u16*)d_in[2];
  p.wq = (const u16*)d_in[3];
  p.wo = (const u16*)d_in[4];
  p.wg = (const u16*)d_in[6];
  p.wu = (const u16*)d_in[7];
  p.wd = (const u16*)d_in[8];
  p.out = (u16*)d_out;
  p.ws = (unsigned char*)d_ws;
  mega_kernel<<<256, NTHR, 0, stream>>>(p);
}
```

```cpp
#include <hip/hip_runtime.h>
#include <hip/hip_bf16.h>
#include <stdint.h>

typedef unsigned v4u __attribute__((ext_vector_type(4)));
typedef float f32x4 __attribute__((ext_vector_type(4)));
typedef float f32x2 __attribute__((ext_vector_type(2)));
typedef unsigned short u16;
using bf16x2 = __attribute__((ext_vector_type(2))) __bf16;

constexpr int D = 576, SK = 32768, DH = 128, W = 4096, F = 4608, NQ = 4096;
constexpr int NTHR = 576;
constexpr int WS_E = 0;
constexpr int WS_QG = 4096;
constexpr int WS_AG = WS_QG + 256 * 512;
constexpr int WS_HG = WS_AG + 256 * 2304;
constexpr int WS_FG = WS_HG + 216 * 1024;
constexpr int WS_END = WS_FG + 256 * 4608;
constexpr int WS_STAMP = 4 << 20;
constexpr unsigned SPIN_CAP = 1u << 16;

#define STAMP(i) do {} while (0)

struct Params {
  const u16 *x, *kc, *vc, *wq, *wo, *wg, *wu, *wd;
  u16* out;
  unsigned char* ws;
};

__device__ __forceinline__ float rbf(float f) {
  unsigned u = __float_as_uint(f);
  u += 0x7FFFu + ((u >> 16) & 1u);
  return __uint_as_float(u & 0xFFFF0000u);
}
__device__ __forceinline__ unsigned f2bf(float f) {
  unsigned u = __float_as_uint(f);
  u += 0x7FFFu + ((u >> 16) & 1u);
  return u >> 16;
}
__device__ __forceinline__ float lo(unsigned w) { return __uint_as_float(w << 16); }
__device__ __forceinline__ float hi(unsigned w) { return __uint_as_float(w & 0xFFFF0000u); }
__device__ __forceinline__ float dot2(unsigned k, unsigned q, float acc) {
  return __builtin_amdgcn_fdot2_f32_bf16(__builtin_bit_cast(bf16x2, k), __builtin_bit_cast(bf16x2, q), acc, false);
}
__device__ __forceinline__ v4u ldw(const u16* p) { return __builtin_nontemporal_load(reinterpret_cast<const v4u*>(p)); }
__device__ __forceinline__ float wave_sum(float v) {
#pragma unroll
  for (int m = 32; m >= 1; m >>= 1) v += __shfl_xor(v, m);
  return v;
}
#define BAR() do { asm volatile("s_waitcnt lgkmcnt(0)" ::: "memory"); __builtin_amdgcn_s_barrier(); asm volatile("" ::: "memory"); } while (0)
#define PIN() __builtin_amdgcn_sched_barrier(0)

template <int H, int M> __device__ __forceinline__ void tr_step(float* v, int lane) {
  const bool up = (lane & M) != 0;
#pragma unroll
  for (int j = 0; j < H; ++j) {
    const float a = v[j], b = v[j + H];
    const float send = up ? a : b, keep = up ? b : a;
    v[j] = keep + __shfl_xor(send, M);
  }
}

__device__ __forceinline__ void fma8(float* acc, float xv, v4u w) {
#pragma unroll
  for (int k = 0; k < 4; ++k) {
    acc[2 * k] = fmaf(xv, lo(w[k]), acc[2 * k]);
    acc[2 * k + 1] = fmaf(xv, hi(w[k]), acc[2 * k + 1]);
  }
}

constexpr int L_XN = 0;
constexpr int L_QP = L_XN + 576;
constexpr int L_QB = L_QP + 576;
constexpr int L_PEX = L_QB + 128;
constexpr int L_MRG = L_PEX + 512;
constexpr int L_GUP = L_MRG + 8 * 264;
constexpr int L_OV = L_GUP + 1024;
constexpr int L_HV = L_OV + 16;
constexpr int L_FIN = L_HV + 32;
constexpr int L_RED = L_FIN + 7 * 576;
constexpr int L_END = L_RED + 160;

__device__ __forceinline__ void ln_qpart(float* lds, int lane, int w, int qrow0, v4u xa, v4u xb, v4u wq0, v4u wq1) {
  const int ch8 = lane & 7, rs8 = lane >> 3;
  float xv[8], xw[8];
#pragma unroll
  for (int k = 0; k < 4; ++k) { xv[2 * k] = lo(xa[k]); xv[2 * k + 1] = hi(xa[k]); xw[2 * k] = lo(xb[k]); xw[2 * k + 1] = hi(xb[k]); }
  const float msk = lane < 8 ? 1.f : 0.f;
  float sm = 0.f, sm2 = 0.f;
#pragma unroll
  for (int k = 0; k < 8; ++k) { sm += xv[k]; sm2 += xw[k]; }
  sm = lane < 8 ? sm + sm2 : sm;
  const float mean = wave_sum(sm) * (1.0f / D);
  float vs = 0.f, vs2 = 0.f;
#pragma unroll
  for (int k = 0; k < 8; ++k) { const float a = xv[k] - mean; vs = fmaf(a, a, vs); const float b = xw[k] - mean; vs2 = fmaf(b, b, vs2); }
  vs = lane < 8 ? vs + vs2 : vs;
  (void)msk;
  const float var = rbf(wave_sum(vs) * (1.0f / D));
  const float mb = rbf(mean);
  const float sd = rbf(sqrtf(rbf(var + 1e-5f)));
  f32x4 o0, o1;
#pragma unroll
  for (int k = 0; k < 4; ++k) { o0[k] = rbf(rbf(xv[k] - mb) / sd); o1[k] = rbf(rbf(xv[4 + k] - mb) / sd); }
  *reinterpret_cast<f32x4*>(&lds[L_XN + 8 * lane]) = o0;
  *reinterpret_cast<f32x4*>(&lds[L_XN + 8 * lane + 4]) = o1;
  if (lane < 8) {
#pragma unroll
    for (int k = 0; k < 4; ++k) { o0[k] = rbf(rbf(xw[k] - mb) / sd); o1[k] = rbf(rbf(xw[4 + k] - mb) / sd); }
    *reinterpret_cast<f32x4*>(&lds[L_XN + 512 + 8 * lane]) = o0;
    *reinterpret_cast<f32x4*>(&lds[L_XN + 512 + 8 * lane + 4]) = o1;
  }
  asm volatile("s_waitcnt lgkmcnt(0)" ::: "memory");
  float acc[8] = {0, 0, 0, 0, 0, 0, 0, 0};
  const float x0 = lds[L_XN + qrow0 + rs8], x1 = lds[L_XN + qrow0 + 8 + rs8];
  fma8(acc, x0, wq0);
  fma8(acc, x1, wq1);
  tr_step<4, 32>(acc, lane); tr_step<2, 16>(acc, lane); tr_step<1, 8>(acc, lane);
  lds[L_QP + 64 * w + 8 * ch8 + rs8] = acc[0];
}

__global__ void __launch_bounds__(NTHR, 3) mega_kernel(Params p) {
  __shared__ __attribute__((aligned(16))) float lds[L_END];
  const int tid = threadIdx.x, lane = tid & 63;
  const int w = __builtin_amdgcn_readfirstlane(tid >> 6);
  const int c = blockIdx.x;
  const int h = c >> 4, s = c & 15;
  const bool heavy = (c & 31) < 27;
  const int hu = heavy ? (c >> 5) * 27 + (c & 31) : 0;
  const int li = (c >> 5) * 5 + ((c & 31) - 27);
  const __amdgpu_buffer_rsrc_t rs = __builtin_amdgcn_make_buffer_rsrc((void*)p.ws, 0, WS_END, 0x00020000);
  const int ch8 = lane & 7, rs8 = lane >> 3;
  const int r4 = lane >> 4, c16 = lane & 15;
  const int qrow0 = 144 * (s >> 2) + 16 * w;
  const u16* wqp = p.wq + (size_t)(qrow0 + rs8) * NQ + 256 * h + 64 * (s & 3) + 8 * ch8;
  STAMP(0);

  if (w == 8) {
    const v4u xa = *reinterpret_cast<const v4u*>(p.x + 8 * lane);
    const v4u xb = *reinterpret_cast<const v4u*>(p.x + 512 + 8 * (lane & 7));
    const v4u wq0 = ldw(wqp), wq1 = ldw(wqp + (size_t)8 * NQ);
    const unsigned e0 = *reinterpret_cast<const unsigned*>(p.ws + WS_E);
    PIN();
    const unsigned tag0 = e0 + 1u, tag1 = ~tag0;
    ln_qpart(lds, lane, w, qrow0, xa, xb, wq0, wq1);
    BAR();
    STAMP(1);
    {
      if (lane < 32) {
        float a = 0.f, b = 0.f;
#pragma unroll
        for (int ww = 0; ww < 9; ++ww) { const f32x2 v = *reinterpret_cast<const f32x2*>(&lds[L_QP + 64 * ww + 2 * lane]); a += v[0]; b += v[1]; }
        const v4u g = {__float_as_uint(a), __float_as_uint(b), tag0, tag1};
        __builtin_amdgcn_raw_buffer_store_b128(g, rs, WS_QG + c * 512 + lane * 16, 0, 16);
      }
    }
    {
      v4u g[8];
      for (unsigned spin = 0;; ++spin) {
        bool ok = true;
#pragma unroll
        for (int k = 0; k < 8; ++k) g[k] = __builtin_amdgcn_raw_buffer_load_b128(rs, WS_QG + (16 * h + (lane >> 5) + 2 * k) * 512 + (lane & 31) * 16, 0, 16);
#pragma unroll
        for (int k = 0; k < 8; ++k) ok &= (g[k][2] == tag0) & (g[k][3] == tag1);
        if (__all(ok) || spin > SPIN_CAP) break;
        asm volatile("" ::: "memory");
      }
      float a[2] = {0.f, 0.f}, b[2] = {0.f, 0.f};
#pragma unroll
      for (int k = 0; k < 8; ++k) { a[k & 1] += __uint_as_float(g[k][0]); b[k & 1] += __uint_as_float(g[k][1]); }
#pragma unroll
      for (int j = 0; j < 2; ++j) {
        const int cg = (lane >> 5) + 2 * j;
        reinterpret_cast<unsigned*>(lds)[L_QB + 32 * cg + (lane & 31)] = f2bf(a[j]) | (f2bf(b[j]) << 16);
      }
    }
    STAMP(2);
    BAR();
    BAR();
    STAMP(3);
    {
#pragma unroll
      for (int g = 0; g < 2; ++g) {
        float mw[8], M = -INFINITY;
#pragma unroll
        for (int ww = 0; ww < 8; ++ww) { mw[ww] = lds[L_MRG + (ww * 2 + g) * 132 + 128]; M = fmaxf(M, mw[ww]); }
        float L = 0.f, a = 0.f, b = 0.f;
#pragma unroll
        for (int ww = 0; ww < 8; ++ww) {
          const float wt = __expf(mw[ww] - M);
          L = fmaf(wt, lds[L_MRG + (ww * 2 + g) * 132 + 129], L);
          const f32x2 v = *reinterpret_cast<const f32x2*>(&lds[L_MRG + (ww * 2 + g) * 132 + 2 * lane]);
          a = fmaf(wt, v[0], a); b = fmaf(wt, v[1], b);
        }
        const int rec = WS_AG + c * 2304 + (g * 8 + (lane >> 3)) * 144;
        const v4u ga = {__float_as_uint(a), __float_as_uint(b), tag0, tag1};
        __builtin_amdgcn_raw_buffer_store_b128(ga, rs, rec + 16 + (lane & 7) * 16, 0, 16);
        if ((lane & 7) == 0) {
          const v4u gm = {__float_as_uint(M), __float_as_uint(L), tag0, tag1};
          __builtin_amdgcn_raw_buffer_store_b128(gm, rs, rec, 0, 16);
        }
      }
    }
    STAMP(4);
    BAR();
    STAMP(5);
    if (heavy) {
      float a = 0.f, b = 0.f;
#pragma unroll
      for (int ww = 0; ww < 8; ++ww) { a += lds[L_GUP + 128 * ww + lane]; b += lds[L_GUP + 128 * ww + 64 + lane]; }
      const v4u g = {__float_as_uint(a), __float_as_uint(b), tag0, tag1};
      __builtin_amdgcn_raw_buffer_store_b128(g, rs, WS_HG + hu * 1024 + lane * 16, 0, 16);
    }
    {
      const int pr = lane >> 2, q4 = lane & 3;
      const int rec = WS_AG + (16 * h + pr) * 2304 + s * 144;
      v4u gm, g0, g1;
      for (unsigned spin = 0;; ++spin) {
        gm = __builtin_amdgcn_raw_buffer_load_b128(rs, rec, 0, 16);
        g0 = __builtin_amdgcn_raw_buffer_load_b128(rs, rec + 16 + 32 * q4, 0, 16);
        g1 = __builtin_amdgcn_raw_buffer_load_b128(rs, rec + 32 + 32 * q4, 0, 16);
        const bool ok = (gm[2] == tag0) & (gm[3] == tag1) & (g0[2] == tag0) & (g0[3] == tag1) & (g1[2] == tag0) & (g1[3] == tag1);
        if (__all(ok) || spin > SPIN_CAP) break;
        asm volatile("" ::: "memory");
      }
      STAMP(6);
      const float m = __uint_as_float(gm[0]);
      float M = m;
#pragma unroll
      for (int k = 4; k <= 32; k <<= 1) M = fmaxf(M, __shfl_xor(M, k));
      const float wt = __expf(m - M);
      float v[5] = {wt * __uint_as_float(gm[1]), wt * __uint_as_float(g0[0]), wt * __uint_as_float(g0[1]), wt * __uint_as_float(g1[0]), wt * __uint_as_float(g1[1])};
#pragma unroll
      for (int j = 0; j < 5; ++j)
#pragma unroll
        for (int k = 4; k <= 32; k <<= 1) v[j] += __shfl_xor(v[j], k);
      if (lane < 4) {
        const float inv = 1.0f / v[0];
        f32x4 o4 = {rbf(v[1] * inv), rbf(v[2] * inv), rbf(v[3] * inv), rbf(v[4] * inv)};
        *reinterpret_cast<f32x4*>(&lds[L_OV + 4 * lane]) = o4;
      }
    }
    {
      const int ks = lane / 18, j = lane - 18 * ks;
      const int f = 18 * c + j;
      const int off = WS_HG + ((ks < 3 ? ks : 0) * 72 + (f >> 6)) * 1024 + (f & 63) * 16;
      v4u g;
      for (unsigned spin = 0;; ++spin) {
        g = __builtin_amdgcn_raw_buffer_load_b128(rs, off, 0, 16);
        const bool ok = (g[2] == tag0) & (g[3] == tag1);
        if (__all(ok) || spin > SPIN_CAP) break;
        asm volatile("" ::: "memory");
      }
      float a = __uint_as_float(g[0]), b = __uint_as_float(g[1]);
      const float a1 = __shfl(a, lane + 18), b1 = __shfl(b, lane + 18), a2 = __shfl(a, lane + 36), b2 = __shfl(b, lane + 36);
      if (lane < 18) {
        float gt = rbf(a + a1 + a2);
        const float up = rbf(b + b1 + b2);
        gt = gt > 0.f ? gt : 0.f;
        lds[L_HV + lane] = rbf(gt * up);
      }
    }
    STAMP(7);
    BAR();
    return;
  }

  const size_t kvbase = ((size_t)h * SK + (SK - W) + 256 * s + 32 * w + r4) * DH + 8 * c16;
  const v4u xa = *reinterpret_cast<const v4u*>(p.x + 8 * lane);
  const v4u xb = *reinterpret_cast<const v4u*>(p.x + 512 + 8 * (lane & 7));
  const v4u wq0 = ldw(wqp), wq1 = ldw(wqp + (size_t)8 * NQ);
  v4u kr[8];
#pragma unroll
  for (int i = 0; i < 8; ++i) kr[i] = ldw(p.kc + kvbase + (size_t)(4 * i) * DH);
  const unsigned e0 = *reinterpret_cast<const unsigned*>(p.ws + WS_E);
  PIN();
  const unsigned tag0 = e0 + 1u, tag1 = ~tag0;
  ln_qpart(lds, lane, w, qrow0, xa, xb, wq0, wq1);
  BAR();
  STAMP(1);
  v4u vr[8];
#pragma unroll
  for (int i = 0; i < 8; ++i) vr[i] = ldw(p.vc + kvbase + (size_t)(4 * i) * DH);
  PIN();
  STAMP(2);
  BAR();
  STAMP(3);
  v4u gr[3], ur[3];
  const int gurow0 = 192 * (hu / 72) + 24 * w;
  {
    const size_t o = heavy ? (size_t)(gurow0 + rs8) * F + 64 * (hu % 72) + 8 * ch8 : (size_t)0;
    const size_t st = heavy ? (size_t)8 * F : (size_t)0;
#pragma unroll
    for (int i = 0; i < 3; ++i) { gr[i] = ldw(p.wg + o + st * i); ur[i] = ldw(p.wu + o + st * i); }
  }
  PIN();
  float pv[16];
  {
    v4u qv[2];
#pragma unroll
    for (int g = 0; g < 2; ++g) qv[g] = *reinterpret_cast<const v4u*>(&lds[L_QB + 64 * g + 4 * c16]);
    float x[16];
#pragma unroll
    for (int i = 0; i < 8; ++i)
#pragma unroll
      for (int g = 0; g < 2; ++g) {
        float d = 0.f;
#pragma unroll
        for (int k = 0; k < 4; ++k) d = dot2(kr[i][k], qv[g][k], d);
        x[2 * i + g] = d;
      }
    tr_step<8, 8>(x, lane); tr_step<4, 4>(x, lane); tr_step<2, 2>(x, lane); tr_step<1, 1>(x, lane);
    const float sc = rbf(rbf(x[0]) * 0.08838834764831845f);
    float m = sc;
#pragma unroll
    for (int k = 2; k <= 32; k <<= 1) m = fmaxf(m, __shfl_xor(m, k));
    const float pe = __expf(sc - m);
    float l = pe;
#pragma unroll
    for (int k = 2; k <= 32; k <<= 1) l += __shfl_xor(l, k);
    if (lane < 2) { lds[L_MRG + (w * 2 + lane) * 132 + 128] = m; lds[L_MRG + (w * 2 + lane) * 132 + 129] = l; }
    lds[L_PEX + 64 * w + lane] = pe;
    asm volatile("s_waitcnt lgkmcnt(0)" ::: "memory");
#pragma unroll
    for (int u = 0; u < 4; ++u) {
      const f32x4 t = *reinterpret_cast<const f32x4*>(&lds[L_PEX + 64 * w + 16 * r4 + 4 * u]);
      pv[4 * u] = t[0]; pv[4 * u + 1] = t[1]; pv[4 * u + 2] = t[2]; pv[4 * u + 3] = t[3];
    }
  }
  STAMP(4);
  const int tq = tid < 504 ? tid : 503;
  const int ch72 = tq % 72, rg7 = tq / 72;
  v4u wor[3], wdr[3];
#pragma unroll
  for (int i = 0; i < 3; ++i) { const int r = min(rg7 + 7 * i, 17); wdr[i] = ldw(p.wd + (size_t)(18 * c + r) * D + 8 * ch72); }
#pragma unroll
  for (int i = 0; i < 3; ++i) { const int r = min(rg7 + 7 * i, 15); wor[i] = ldw(p.wo + (size_t)(16 * c + r) * D + 8 * ch72); }
  PIN();
  {
    float acc[16];
#pragma unroll
    for (int k = 0; k < 16; ++k) acc[k] = 0.f;
#pragma unroll
    for (int i = 0; i < 8; ++i) {
      fma8(acc, pv[2 * i], vr[i]);
      fma8(acc + 8, pv[2 * i + 1], vr[i]);
    }
    tr_step<8, 32>(acc, lane); tr_step<4, 16>(acc, lane);
    const f32x4 o4 = {acc[0], acc[1], acc[2], acc[3]};
    *reinterpret_cast<f32x4*>(&lds[L_MRG + (w * 2 + (lane >> 5)) * 132 + 8 * c16 + 4 * ((lane >> 4) & 1)]) = o4;
  }
  STAMP(5);
  BAR();
  {
    float ag[8] = {0, 0, 0, 0, 0, 0, 0, 0}, au[8] = {0, 0, 0, 0, 0, 0, 0, 0};
#pragma unroll
    for (int i = 0; i < 3; ++i) {
      const float xv = lds[L_XN + gurow0 + 8 * i + rs8];
      fma8(ag, xv, gr[i]);
      fma8(au, xv, ur[i]);
    }
    tr_step<4, 32>(ag, lane); tr_step<2, 16>(ag, lane); tr_step<1, 8>(ag, lane);
    tr_step<4, 32>(au, lane); tr_step<2, 16>(au, lane); tr_step<1, 8>(au, lane);
    lds[L_GUP + 128 * w + 8 * ch8 + rs8] = ag[0];
    lds[L_GUP + 128 * w + 64 + 8 * ch8 + rs8] = au[0];
  }
  STAMP(6);
  BAR();
  BAR();
  STAMP(7);
  if (tid < 504) {
    float acc[8] = {0, 0, 0, 0, 0, 0, 0, 0};
#pragma unroll
    for (int i = 0; i < 3; ++i) {
      const int r = rg7 + 7 * i;
      const float hv = r < 18 ? lds[L_HV + min(r, 17)] : 0.f, ov = r < 16 ? lds[L_OV + min(r, 15)] : 0.f;
      fma8(acc, hv, wdr[i]);
      fma8(acc, ov, wor[i]);
    }
    const f32x4 a0 = {acc[0], acc[1], acc[2], acc[3]}, a1 = {acc[4], acc[5], acc[6], acc[7]};
    *reinterpret_cast<f32x4*>(&lds[L_FIN + 576 * rg7 + 8 * ch72]) = a0;
    *reinterpret_cast<f32x4*>(&lds[L_FIN + 576 * rg7 + 8 * ch72 + 4]) = a1;
  }
  BAR();
  if (tid < 288) {
    float a = 0.f, b = 0.f;
#pragma unroll
    for (int r = 0; r < 7; ++r) { const f32x2 v = *reinterpret_cast<const f32x2*>(&lds[L_FIN + 576 * r + 2 * tid]); a += v[0]; b += v[1]; }
    const v4u g = {__float_as_uint(a), __float_as_uint(b), tag0, tag1};
    __builtin_amdgcn_raw_buffer_store_b128(g, rs, WS_FG + c * 4608 + tid * 16, 0, 16);
  }
  STAMP(8);
  if (heavy || li >= 36) return;
  {
    const int gi = tid & 7, p0 = tid >> 3;
    v4u g[4];
    for (unsigned spin = 0;; ++spin) {
      bool ok = true;
#pragma unroll
      for (int k = 0; k < 4; ++k) g[k] = __builtin_amdgcn_raw_buffer_load_b128(rs, WS_FG + (p0 + 64 * k) * 4608 + li * 128 + gi * 16, 0, 16);
#pragma unroll
      for (int k = 0; k < 4; ++k) ok &= (g[k][2] == tag0) & (g[k][3] == tag1);
      if (__all(ok) || spin > SPIN_CAP) break;
      asm volatile("" ::: "memory");
    }
    STAMP(9);
    float a = 0.f, b = 0.f;
#pragma unroll
    for (int k = 0; k < 4; ++k) { a += __uint_as_float(g[k][0]); b += __uint_as_float(g[k][1]); }
#pragma unroll
    for (int k = 8; k <= 32; k <<= 1) { a += __shfl_xor(a, k); b += __shfl_xor(b, k); }
    if (lane < 8) { lds[L_RED + 16 * w + 2 * lane] = a; lds[L_RED + 16 * w + 2 * lane + 1] = b; }
    BAR();
    if (tid < 16) {
      float t = 0.f;
#pragma unroll
      for (int ww = 0; ww < 8; ++ww) t += lds[L_RED + 16 * ww + tid];
      const int n = 16 * li + tid;
      const float xv = __uint_as_float((unsigned)p.x[n] << 16);
      p.out[n] = (u16)f2bf(xv + t);
    }
    if (li == 0 && tid == 0) *reinterpret_cast<unsigned*>(p.ws + WS_E) = tag0;
    STAMP(10);
  }
}

extern "C" void kernel_launch(void* const* d_in, const int* in_sizes, int n_in, void* d_out, int out_size, void* d_ws, size_t ws_size,
                              hipStream_t stream) {
  Params p;
  p.x = (const u16*)d_in[0];
  p.kc = (const u16*)d_in[1];
  p.vc = (const u16*)d_in[2];
  p.wq = (const u16*)d_in[3];
  p.wo = (const u16*)d_in[4];
  p.wg = (const u16*)d_in[6];
  p.wu = (const u16*)d_in[7];
  p.wd = (const u16*)d_in[8];
  p.out = (u16*)d_out;
  p.ws = (unsigned char*)d_ws;
  mega_kernel<<<256, NTHR, 0, stream>>>(p);
}
```

```cpp
#include <hip/hip_runtime.h>
#include <hip/hip_bf16.h>
#include <stdint.h>

typedef unsigned v4u __attribute__((ext_vector_type(4)));
typedef float f32x4 __attribute__((ext_vector_type(4)));
typedef float f32x2 __attribute__((ext_vector_type(2)));
typedef unsigned short u16;
using bf16x2 = __attribute__((ext_vector_type(2))) __bf16;

constexpr int D = 576, SK = 32768, DH = 128, W = 4096, F = 4608, NQ = 4096;
constexpr int NTHR = 576;
#define ORDER 0
constexpr int WS_E = 0;
constexpr int WS_QG = 4096;
constexpr int WS_AG = WS_QG + 256 * 512;
constexpr int WS_HG = WS_AG + 256 * 2304;
constexpr int WS_FG = WS_HG + 216 * 1024;
constexpr int WS_END = WS_FG + 256 * 4608;
constexpr int WS_STAMP = 4 << 20;
constexpr unsigned SPIN_CAP = 1u << 16;

#define STAMP(i) do {} while (0)

struct Params {
  const u16 *x, *kc, *vc, *wq, *wo, *wg, *wu, *wd;
  u16* out;
  unsigned char* ws;
};

__device__ __forceinline__ float rbf(float f) {
  unsigned u = __float_as_uint(f);
  u += 0x7FFFu + ((u >> 16) & 1u);
  return __uint_as_float(u & 0xFFFF0000u);
}
__device__ __forceinline__ unsigned f2bf(float f) {
  unsigned u = __float_as_uint(f);
  u += 0x7FFFu + ((u >> 16) & 1u);
  return u >> 16;
}
__device__ __forceinline__ float lo(unsigned w) { return __uint_as_float(w << 16); }
__device__ __forceinline__ float hi(unsigned w) { return __uint_as_float(w & 0xFFFF0000u); }
__device__ __forceinline__ float dot2(unsigned k, unsigned q, float acc) {
  return __builtin_amdgcn_fdot2_f32_bf16(__builtin_bit_cast(bf16x2, k), __builtin_bit_cast(bf16x2, q), acc, false);
}
__device__ __forceinline__ v4u ldw(const u16* p) { return __builtin_nontemporal_load(reinterpret_cast<const v4u*>(p)); }

template <int CTRL> __device__ __forceinline__ float dpp(float x) {
  return __builtin_bit_cast(float, __builtin_amdgcn_mov_dpp(__builtin_bit_cast(int, x), CTRL, 0xf, 0xf, true));
}
constexpr int XOR1 = 0xB1, XOR2 = 0x4E, XOR7 = 0x141, XOR8 = 0x128, ROR4 = 0x124, MIR = 0x140;
__device__ __forceinline__ float sw32_sum(float a, float b) {
  auto r = __builtin_amdgcn_permlane32_swap(__float_as_uint(a), __float_as_uint(b), false, false);
  return __uint_as_float(r[0]) + __uint_as_float(r[1]);
}
__device__ __forceinline__ float sw16_sum(float a, float b) {
  auto r = __builtin_amdgcn_permlane16_swap(__float_as_uint(a), __float_as_uint(b), false, false);
  return __uint_as_float(r[0]) + __uint_as_float(r[1]);
}
__device__ __forceinline__ float sw32_max(float a) {
  auto r = __builtin_amdgcn_permlane32_swap(__float_as_uint(a), __float_as_uint(a), false, false);
  return fmaxf(__uint_as_float(r[0]), __uint_as_float(r[1]));
}
__device__ __forceinline__ float sw16_max(float a) {
  auto r = __builtin_amdgcn_permlane16_swap(__float_as_uint(a), __float_as_uint(a), false, false);
  return fmaxf(__uint_as_float(r[0]), __uint_as_float(r[1]));
}
__device__ __forceinline__ float wave_sum(float v) {
  v += dpp<XOR1>(v); v += dpp<XOR2>(v); v += dpp<XOR7>(v); v += dpp<XOR8>(v);
  v = sw16_sum(v, v); v = sw32_sum(v, v);
  return v;
}
template <int H> __device__ __forceinline__ void tr32(float* v) {
#pragma unroll
  for (int j = 0; j < H; ++j) v[j] = sw32_sum(v[j], v[j + H]);
}
template <int H> __device__ __forceinline__ void tr16(float* v) {
#pragma unroll
  for (int j = 0; j < H; ++j) v[j] = sw16_sum(v[j], v[j + H]);
}
template <int H, int CTRL, int BIT> __device__ __forceinline__ void trd(float* v, int lane) {
#pragma unroll
  for (int j = 0; j < H; ++j) {
    const float t1 = v[j] + dpp<CTRL>(v[j]), t2 = v[j + H] + dpp<CTRL>(v[j + H]);
    v[j] = (lane & BIT) ? t2 : t1;
  }
}
#define BAR() do { asm volatile("s_waitcnt lgkmcnt(0)" ::: "memory"); __builtin_amdgcn_s_barrier(); asm volatile("" ::: "memory"); } while (0)
#define PIN() __builtin_amdgcn_sched_barrier(0)

__device__ __forceinline__ void fma8(float* acc, float xv, v4u w) {
#pragma unroll
  for (int k = 0; k < 4; ++k) {
    acc[2 * k] = fmaf(xv, lo(w[k]), acc[2 * k]);
    acc[2 * k + 1] = fmaf(xv, hi(w[k]), acc[2 * k + 1]);
  }
}

constexpr int L_XN = 0;
constexpr int L_QP = L_XN + 576;
constexpr int L_QB = L_QP + 576;
constexpr int L_PEX = L_QB + 128;
constexpr int L_MRG = L_PEX + 512;
constexpr int L_GUP = L_MRG + 8 * 264;
constexpr int L_OV = L_GUP + 1024;
constexpr int L_HV = L_OV + 16;
constexpr int L_FIN = L_HV + 32;
constexpr int L_RED = L_FIN + 7 * 576;
constexpr int L_END = L_RED + 160;

__device__ __forceinline__ float divr(float a, float b, float ib) { const float q = a * ib; const float r = fmaf(-q, b, a); return fmaf(r, ib, q); }
struct LnS { float mb, sd, isd; };
__device__ __forceinline__ LnS ln_stats(int lane, v4u xa, v4u xb) {
  float xv[8], xw[8];
#pragma unroll
  for (int k = 0; k < 4; ++k) { xv[2 * k] = lo(xa[k]); xv[2 * k + 1] = hi(xa[k]); xw[2 * k] = lo(xb[k]); xw[2 * k + 1] = hi(xb[k]); }
  float sm = 0.f, sm2 = 0.f;
#pragma unroll
  for (int k = 0; k < 8; ++k) { sm += xv[k]; sm2 += xw[k]; }
  sm = lane < 8 ? sm + sm2 : sm;
  const float mean = wave_sum(sm) * (1.0f / D);
  float vs = 0.f, vs2 = 0.f;
#pragma unroll
  for (int k = 0; k < 8; ++k) { const float a = xv[k] - mean; vs = fmaf(a, a, vs); const float b = xw[k] - mean; vs2 = fmaf(b, b, vs2); }
  vs = lane < 8 ? vs + vs2 : vs;
  const float var = rbf(wave_sum(vs) * (1.0f / D));
  LnS r;
  r.mb = rbf(mean);
  r.sd = rbf(sqrtf(rbf(var + 1e-5f)));
  r.isd = 1.0f / r.sd;
  return r;
}
__device__ __forceinline__ float ln_apply(float x, const LnS& s) { return rbf(divr(rbf(x - s.mb), s.sd, s.isd)); }
__device__ __forceinline__ void q_part(float* lds, int lane, int w, const LnS& s, unsigned xq0, unsigned xq1, v4u wq0, v4u wq1) {
  float acc[8] = {0, 0, 0, 0, 0, 0, 0, 0};
  fma8(acc, ln_apply(__uint_as_float(xq0 << 16), s), wq0);
  fma8(acc, ln_apply(__uint_as_float(xq1 << 16), s), wq1);
  tr32<4>(acc); tr16<2>(acc); trd<1, XOR8, 8>(acc, lane);
  lds[L_QP + 64 * w + 8 * (lane & 7) + (lane >> 3)] = acc[0];
}
__device__ __forceinline__ void ln_store8(float* dst, v4u xa, const LnS& s) {
  f32x4 o0, o1;
#pragma unroll
  for (int k = 0; k < 2; ++k) { o0[2 * k] = ln_apply(lo(xa[k]), s); o0[2 * k + 1] = ln_apply(hi(xa[k]), s); o1[2 * k] = ln_apply(lo(xa[2 + k]), s); o1[2 * k + 1] = ln_apply(hi(xa[2 + k]), s); }
  *reinterpret_cast<f32x4*>(dst) = o0;
  *reinterpret_cast<f32x4*>(dst + 4) = o1;
}

__global__ void __launch_bounds__(NTHR, 3) mega_kernel(Params p) {
  __shared__ __attribute__((aligned(16))) float lds[L_END];
  const int tid = threadIdx.x, lane = tid & 63;
  const int w = __builtin_amdgcn_readfirstlane(tid >> 6);
  const int c = blockIdx.x;
  const int h = c >> 4, s = c & 15;
  const bool heavy = (c & 31) < 27;
  const int hu = heavy ? (c >> 5) * 27 + (c & 31) : 0;
  const int li = (c >> 5) * 5 + ((c & 31) - 27);
  const __amdgpu_buffer_rsrc_t rs = __builtin_amdgcn_make_buffer_rsrc((void*)p.ws, 0, WS_END, 0x00020000);
  const int ch8 = lane & 7, rs8 = lane >> 3;
  const int r4 = lane >> 4, c16 = lane & 15;
  const int qrow0 = 144 * (s >> 2) + 16 * w;
  const u16* wqp = p.wq + (size_t)(qrow0 + rs8) * NQ + 256 * h + 64 * (s & 3) + 8 * ch8;
  STAMP(0);
  unsigned cpf = 0;
  unsigned cpt[13];

  if (w == 8) {
    __builtin_amdgcn_s_setprio(3);
    const v4u xa = *reinterpret_cast<const v4u*>(p.x + 8 * lane);
    const v4u xb = *reinterpret_cast<const v4u*>(p.x + 512 + 8 * (lane & 7));
    const unsigned xq0 = p.x[qrow0 + rs8], xq1 = p.x[qrow0 + 8 + rs8];
    const v4u wq0 = ldw(wqp), wq1 = ldw(wqp + (size_t)8 * NQ);
    const unsigned e0 = *reinterpret_cast<const unsigned*>(p.ws + WS_E);
    PIN();
    __builtin_amdgcn_s_barrier();
    PIN();
    const unsigned tag0 = e0 + 1u, tag1 = ~tag0;
    const LnS lns = ln_stats(lane, xa, xb);
    STAMP(12);
    q_part(lds, lane, w, lns, xq0, xq1, wq0, wq1);
    STAMP(13);
    BAR();
    STAMP(1);
    {
      if (lane < 32) {
        float a = 0.f, b = 0.f;
#pragma unroll
        for (int ww = 0; ww < 9; ++ww) { const f32x2 v = *reinterpret_cast<const f32x2*>(&lds[L_QP + 64 * ww + 2 * lane]); a += v[0]; b += v[1]; }
        const v4u g = {__float_as_uint(a), __float_as_uint(b), tag0, tag1};
        __builtin_amdgcn_raw_buffer_store_b128(g, rs, WS_QG + c * 512 + lane * 16, 0, 16);
      }
    }
    {
      v4u g[8];
      for (unsigned spin = 0;; ++spin) {
        bool ok = true;
#pragma unroll
        for (int k = 0; k < 8; ++k) g[k] = __builtin_amdgcn_raw_buffer_load_b128(rs, WS_QG + (16 * h + (lane >> 5) + 2 * k) * 512 + (lane & 31) * 16, 0, 16);
#pragma unroll
        for (int k = 0; k < 8; ++k) ok &= (g[k][2] == tag0) & (g[k][3] == tag1);
        if (__all(ok) || spin > SPIN_CAP) break;
        asm volatile("" ::: "memory");
      }
      float a[2] = {0.f, 0.f}, b[2] = {0.f, 0.f};
#pragma unroll
      for (int k = 0; k < 8; ++k) { a[k & 1] += __uint_as_float(g[k][0]); b[k & 1] += __uint_as_float(g[k][1]); }
#pragma unroll
      for (int j = 0; j < 2; ++j) {
        const int cg = (lane >> 5) + 2 * j;
        reinterpret_cast<unsigned*>(lds)[L_QB + 32 * cg + (lane & 31)] = f2bf(a[j]) | (f2bf(b[j]) << 16);
      }
    }
    STAMP(2);
    BAR();
    BAR();
    STAMP(3);
    {
#pragma unroll
      for (int g = 0; g < 2; ++g) {
        float mw[8], M = -INFINITY;
#pragma unroll
        for (int ww = 0; ww < 8; ++ww) { mw[ww] = lds[L_MRG + (ww * 2 + g) * 132 + 128]; M = fmaxf(M, mw[ww]); }
        float L = 0.f, a = 0.f, b = 0.f;
#pragma unroll
        for (int ww = 0; ww < 8; ++ww) {
          const float wt = __expf(mw[ww] - M);
          L = fmaf(wt, lds[L_MRG + (ww * 2 + g) * 132 + 129], L);
          const f32x2 v = *reinterpret_cast<const f32x2*>(&lds[L_MRG + (ww * 2 + g) * 132 + 2 * lane]);
          a = fmaf(wt, v[0], a); b = fmaf(wt, v[1], b);
        }
        const int rec = WS_AG + c * 2304 + (g * 8 + (lane >> 3)) * 144;
        const v4u ga = {__float_as_uint(a), __float_as_uint(b), tag0, tag1};
        __builtin_amdgcn_raw_buffer_store_b128(ga, rs, rec + 16 + (lane & 7) * 16, 0, 16);
        if ((lane & 7) == 0) {
          const v4u gm = {__float_as_uint(M), __float_as_uint(L), tag0, tag1};
          __builtin_amdgcn_raw_buffer_store_b128(gm, rs, rec, 0, 16);
        }
      }
    }
    STAMP(4);
    BAR();
    STAMP(5);
    if (heavy) {
      float a = 0.f, b = 0.f;
#pragma unroll
      for (int ww = 0; ww < 8; ++ww) { a += lds[L_GUP + 128 * ww + lane]; b += lds[L_GUP + 128 * ww + 64 + lane]; }
      const v4u g = {__float_as_uint(a), __float_as_uint(b), tag0, tag1};
      __builtin_amdgcn_raw_buffer_store_b128(g, rs, WS_HG + hu * 1024 + lane * 16, 0, 16);
    }
    {
      const int pr = lane >> 2, q4 = lane & 3;
      const int rec = WS_AG + (16 * h + pr) * 2304 + s * 144;
      v4u gm, g0, g1;
      for (unsigned spin = 0;; ++spin) {
        gm = __builtin_amdgcn_raw_buffer_load_b128(rs, rec, 0, 16);
        g0 = __builtin_amdgcn_raw_buffer_load_b128(rs, rec + 16 + 32 * q4, 0, 16);
        g1 = __builtin_amdgcn_raw_buffer_load_b128(rs, rec + 32 + 32 * q4, 0, 16);
        const bool ok = (gm[2] == tag0) & (gm[3] == tag1) & (g0[2] == tag0) & (g0[3] == tag1) & (g1[2] == tag0) & (g1[3] == tag1);
        if (__all(ok) || spin > SPIN_CAP) break;
        asm volatile("" ::: "memory");
      }
      STAMP(6);
      const float m = __uint_as_float(gm[0]);
      float M = m;
#pragma unroll
      for (int k = 4; k <= 32; k <<= 1) M = fmaxf(M, __shfl_xor(M, k));
      const float wt = __expf(m - M);
      float v[5] = {wt * __uint_as_float(gm[1]), wt * __uint_as_float(g0[0]), wt * __uint_as_float(g0[1]), wt * __uint_as_float(g1[0]), wt * __uint_as_float(g1[1])};
#pragma unroll
      for (int j = 0; j < 5; ++j)
#pragma unroll
        for (int k = 4; k <= 32; k <<= 1) v[j] += __shfl_xor(v[j], k);
      if (lane < 4) {
        const float inv = 1.0f / v[0];
        f32x4 o4 = {rbf(v[1] * inv), rbf(v[2] * inv), rbf(v[3] * inv), rbf(v[4] * inv)};
        *reinterpret_cast<f32x4*>(&lds[L_OV + 4 * lane]) = o4;
      }
    }
    {
      const int ks = lane / 18, j = lane - 18 * ks;
      const int f = 18 * c + j;
      const int off = WS_HG + ((ks < 3 ? ks : 0) * 72 + (f >> 6)) * 1024 + (f & 63) * 16;
      v4u g;
      for (unsigned spin = 0;; ++spin) {
        g = __builtin_amdgcn_raw_buffer_load_b128(rs, off, 0, 16);
        const bool ok = (g[2] == tag0) & (g[3] == tag1);
        if (__all(ok) || spin > SPIN_CAP) break;
        asm volatile("" ::: "memory");
      }
      float a = __uint_as_float(g[0]), b = __uint_as_float(g[1]);
      const float a1 = __shfl(a, lane + 18), b1 = __shfl(b, lane + 18), a2 = __shfl(a, lane + 36), b2 = __shfl(b, lane + 36);
      if (lane < 18) {
        float gt = rbf(a + a1 + a2);
        const float up = rbf(b + b1 + b2);
        gt = gt > 0.f ? gt : 0.f;
        lds[L_HV + lane] = rbf(gt * up);
      }
    }
    STAMP(7);
    asm volatile("" :: "v"(cpf));
    BAR();
    return;
  }

  const size_t kvbase = ((size_t)h * SK + (SK - W) + 256 * s + 32 * w + r4) * DH + 8 * c16;
  const v4u xa = *reinterpret_cast<const v4u*>(p.x + 8 * lane);
  const v4u xb = *reinterpret_cast<const v4u*>(p.x + 512 + 8 * (lane & 7));
  const unsigned xq0 = p.x[qrow0 + rs8], xq1 = p.x[qrow0 + 8 + rs8];
  const v4u wq0 = ldw(wqp), wq1 = ldw(wqp + (size_t)8 * NQ);
  const unsigned e0 = *reinterpret_cast<const unsigned*>(p.ws + WS_E);
  PIN();
  __builtin_amdgcn_s_barrier();
  PIN();
  v4u kr[8], vr[8], gr[3], ur[3], wor[3], wdr[3];
  const int gurow0 = 192 * (hu / 72) + 24 * w;
  const size_t guo = heavy ? (size_t)(gurow0 + rs8) * F + 64 * (hu % 72) + 8 * ch8 : (size_t)0;
  const size_t gust = heavy ? (size_t)8 * F : (size_t)0;
  const int tq = tid < 504 ? tid : 503;
  const int ch72 = tq % 72, rg7 = tq / 72;
#define DEPTH 63
#define STR2(x) #x
#define STR(x) STR2(x)
#define PACE() do { PIN(); asm volatile("s_waitcnt vmcnt(" STR(DEPTH) ")" ::: "memory"); PIN(); } while (0)
#define LD_K0() _Pragma("unroll") for (int i = 0; i < 8; ++i) kr[i] = ldw(p.kc + kvbase + (size_t)(4 * i) * DH);
#define LD_GU0() _Pragma("unroll") for (int i = 0; i < 3; ++i) { gr[i] = ldw(p.wg + guo + gust * i); ur[i] = ldw(p.wu + guo + gust * i); }
#define LD_K() _Pragma("unroll") for (int i = 0; i < 8; ++i) { PACE(); kr[i] = ldw(p.kc + kvbase + (size_t)(4 * i) * DH); }
#define LD_V() _Pragma("unroll") for (int i = 0; i < 8; ++i) { PACE(); vr[i] = ldw(p.vc + kvbase + (size_t)(4 * i) * DH); }
#define LD_GU() _Pragma("unroll") for (int i = 0; i < 3; ++i) { PACE(); gr[i] = ldw(p.wg + guo + gust * i); PACE(); ur[i] = ldw(p.wu + guo + gust * i); }
#define LD_DO() _Pragma("unroll") for (int i = 0; i < 3; ++i) { const int r = min(rg7 + 7 * i, 17); PACE(); wdr[i] = ldw(p.wd + (size_t)(18 * c + r) * D + 8 * ch72); } \
                _Pragma("unroll") for (int i = 0; i < 3; ++i) { const int r = min(rg7 + 7 * i, 15); PACE(); wor[i] = ldw(p.wo + (size_t)(16 * c + r) * D + 8 * ch72); }
  LD_K0();
  PIN();
  const unsigned tag0 = e0 + 1u, tag1 = ~tag0;
  const LnS lns = ln_stats(lane, xa, xb);
  STAMP(12);
  q_part(lds, lane, w, lns, xq0, xq1, wq0, wq1);
  STAMP(13);
  BAR();
  STAMP(1);
  LD_V(); PIN(); LD_GU(); PIN(); LD_DO();
  PIN();
  if ((lane >> 3) == w) ln_store8(&lds[L_XN + 8 * lane], xa, lns);
  if (w == 7 && lane < 8) ln_store8(&lds[L_XN + 512 + 8 * lane], xb, lns);
  STAMP(2);
  BAR();
  STAMP(3);
  float pv[16];
  {
    v4u qv[2];
#pragma unroll
    for (int g = 0; g < 2; ++g) qv[g] = *reinterpret_cast<const v4u*>(&lds[L_QB + 64 * g + 4 * c16]);
    float x[16];
#pragma unroll
    for (int i = 0; i < 8; ++i)
#pragma unroll
      for (int g = 0; g < 2; ++g) {
        float d = 0.f;
#pragma unroll
        for (int k = 0; k < 4; ++k) d = dot2(kr[i][k], qv[g][k], d);
        x[2 * i + g] = d;
      }
    trd<8, XOR8, 8>(x, lane); trd<4, XOR7, 4>(x, lane); trd<2, XOR2, 2>(x, lane); trd<1, XOR1, 1>(x, lane);
    const float sc = rbf(rbf(x[0]) * 0.08838834764831845f);
    float m = sc;
    m = fmaxf(m, dpp<XOR2>(m)); m = fmaxf(m, dpp<ROR4>(m)); m = fmaxf(m, dpp<XOR8>(m)); m = sw16_max(m); m = sw32_max(m);
    const float pe = __expf(sc - m);
    float l = pe;
    l += dpp<XOR2>(l); l += dpp<ROR4>(l); l += dpp<XOR8>(l); l = sw16_sum(l, l); l = sw32_sum(l, l);
    if (lane < 2) { lds[L_MRG + (w * 2 + lane) * 132 + 128] = m; lds[L_MRG + (w * 2 + lane) * 132 + 129] = l; }
    lds[L_PEX + 64 * w + lane] = pe;
    asm volatile("s_waitcnt lgkmcnt(0)" ::: "memory");
#pragma unroll
    for (int u = 0; u < 4; ++u) {
      const f32x4 t = *reinterpret_cast<const f32x4*>(&lds[L_PEX + 64 * w + 16 * r4 + 4 * u]);
      pv[4 * u] = t[0]; pv[4 * u + 1] = t[1]; pv[4 * u + 2] = t[2]; pv[4 * u + 3] = t[3];
    }
  }
  STAMP(4);
  {
    float acc[16];
#pragma unroll
    for (int k = 0; k < 16; ++k) acc[k] = 0.f;
#pragma unroll
    for (int i = 0; i < 8; ++i) {
      fma8(acc, pv[2 * i], vr[i]);
      fma8(acc + 8, pv[2 * i + 1], vr[i]);
    }
    tr32<8>(acc); tr16<4>(acc);
    const f32x4 o4 = {acc[0], acc[1], acc[2], acc[3]};
    *reinterpret_cast<f32x4*>(&lds[L_MRG + (w * 2 + (lane >> 5)) * 132 + 8 * c16 + 4 * ((lane >> 4) & 1)]) = o4;
  }
  STAMP(5);
  BAR();
  {
    float ag[8] = {0, 0, 0, 0, 0, 0, 0, 0}, au[8] = {0, 0, 0, 0, 0, 0, 0, 0};
#pragma unroll
    for (int i = 0; i < 3; ++i) {
      const float xv = lds[L_XN + gurow0 + 8 * i + rs8];
      fma8(ag, xv, gr[i]);
      fma8(au, xv, ur[i]);
    }
    tr32<4>(ag); tr16<2>(ag); trd<1, XOR8, 8>(ag, lane);
    tr32<4>(au); tr16<2>(au); trd<1, XOR8, 8>(au, lane);
    lds[L_GUP + 128 * w + 8 * ch8 + rs8] = ag[0];
    lds[L_GUP + 128 * w + 64 + 8 * ch8 + rs8] = au[0];
  }
  STAMP(6);
  BAR();
  BAR();
  STAMP(7);
  if (tid < 504) {
    float acc[8] = {0, 0, 0, 0, 0, 0, 0, 0};
#pragma unroll
    for (int i = 0; i < 3; ++i) {
      const int r = rg7 + 7 * i;
      const float hv = r < 18 ? lds[L_HV + min(r, 17)] : 0.f, ov = r < 16 ? lds[L_OV + min(r, 15)] : 0.f;
      fma8(acc, hv, wdr[i]);
      fma8(acc, ov, wor[i]);
    }
    const f32x4 a0 = {acc[0], acc[1], acc[2], acc[3]}, a1 = {acc[4], acc[5], acc[6], acc[7]};
    *reinterpret_cast<f32x4*>(&lds[L_FIN + 576 * rg7 + 8 * ch72]) = a0;
    *reinterpret_cast<f32x4*>(&lds[L_FIN + 576 * rg7 + 8 * ch72 + 4]) = a1;
  }
  BAR();
  if (tid < 288) {
    float a = 0.f, b = 0.f;
#pragma unroll
    for (int r = 0; r < 7; ++r) { const f32x2 v = *reinterpret_cast<const f32x2*>(&lds[L_FIN + 576 * r + 2 * tid]); a += v[0]; b += v[1]; }
    const v4u g = {__float_as_uint(a), __float_as_uint(b), tag0, tag1};
    __builtin_amdgcn_raw_buffer_store_b128(g, rs, WS_FG + c * 4608 + tid * 16, 0, 16);
  }
  STAMP(8);
  if (heavy || li >= 36) return;
  {
    const int gi = tid & 7, p0 = tid >> 3;
    v4u g[4];
    for (unsigned spin = 0;; ++spin) {
      bool ok = true;
#pragma unroll
      for (int k = 0; k < 4; ++k) g[k] = __builtin_amdgcn_raw_buffer_load_b128(rs, WS_FG + (p0 + 64 * k) * 4608 + li * 128 + gi * 16, 0, 16);
#pragma unroll
      for (int k = 0; k < 4; ++k) ok &= (g[k][2] == tag0) & (g[k][3] == tag1);
      if (__all(ok) || spin > SPIN_CAP) break;
      asm volatile("" ::: "memory");
    }
    STAMP(9);
    float a = 0.f, b = 0.f;
#pragma unroll
    for (int k = 0; k < 4; ++k) { a += __uint_as_float(g[k][0]); b += __uint_as_float(g[k][1]); }
#pragma unroll
    for (int k = 8; k <= 32; k <<= 1) { a += __shfl_xor(a, k); b += __shfl_xor(b, k); }
    if (lane < 8) { lds[L_RED + 16 * w + 2 * lane] = a; lds[L_RED + 16 * w + 2 * lane + 1] = b; }
    BAR();
    if (tid < 16) {
      float t = 0.f;
#pragma unroll
      for (int ww = 0; ww < 8; ++ww) t += lds[L_RED + 16 * ww + tid];
      const int n = 16 * li + tid;
      const float xv = __uint_as_float((unsigned)p.x[n] << 16);
      p.out[n] = (u16)f2bf(xv + t);
    }
    if (li == 0 && tid == 0) *reinterpret_cast<unsigned*>(p.ws + WS_E) = tag0;
    STAMP(10);
  }
}

extern "C" void kernel_launch(void* const* d_in, const int* in_sizes, int n_in, void* d_out, int out_size, void* d_ws, size_t ws_size,
                              hipStream_t stream) {
  Params p;
  p.x = (const u16*)d_in[0];
  p.kc = (const u16*)d_in[1];
  p.vc = (const u16*)d_in[2];
  p.wq = (const u16*)d_in[3];
  p.wo = (const u16*)d_in[4];
  p.wg = (const u16*)d_in[6];
  p.wu = (const u16*)d_in[7];
  p.wd = (const u16*)d_in[8];
  p.out = (u16*)d_out;
  p.ws = (unsigned char*)d_ws;
  mega_kernel<<<256, NTHR, 0, stream>>>(p);
}
```

```cpp
#include <hip/hip_runtime.h>
#include <hip/hip_bf16.h>
#include <stdint.h>

typedef unsigned v4u __attribute__((ext_vector_type(4)));
typedef float f32x4 __attribute__((ext_vector_type(4)));
typedef float f32x2 __attribute__((ext_vector_type(2)));
typedef unsigned short u16;
using bf16x2 = __attribute__((ext_vector_type(2))) __bf16;

constexpr int D = 576, SK = 32768, DH = 128, W = 4096, F = 4608, NQ = 4096;
#define TLBW 1
constexpr int NTHR = 576;
#define ORDER 0
constexpr int WS_E = 0;
constexpr int WS_QG = 4096;
constexpr int WS_AG = WS_QG + 256 * 512;
constexpr int WS_HG = WS_AG + 256 * 2304;
constexpr int WS_FG = WS_HG + 216 * 1024;
constexpr int WS_END = WS_FG + 256 * 4608;
constexpr int WS_STAMP = 4 << 20;
constexpr unsigned SPIN_CAP = 1u << 16;

#define STAMP(i) do {} while (0)

struct Params {
  const u16 *x, *kc, *vc, *wq, *wo, *wg, *wu, *wd;
  u16* out;
  unsigned char* ws;
};

__device__ __forceinline__ float rbf(float f) {
  unsigned u = __float_as_uint(f);
  u += 0x7FFFu + ((u >> 16) & 1u);
  return __uint_as_float(u & 0xFFFF0000u);
}
__device__ __forceinline__ unsigned f2bf(float f) {
  unsigned u = __float_as_uint(f);
  u += 0x7FFFu + ((u >> 16) & 1u);
  return u >> 16;
}
__device__ __forceinline__ float lo(unsigned w) { return __uint_as_float(w << 16); }
__device__ __forceinline__ float hi(unsigned w) { return __uint_as_float(w & 0xFFFF0000u); }
__device__ __forceinline__ float dot2(unsigned k, unsigned q, float acc) {
  return __builtin_amdgcn_fdot2_f32_bf16(__builtin_bit_cast(bf16x2, k), __builtin_bit_cast(bf16x2, q), acc, false);
}
__device__ __forceinline__ v4u ldw(const u16* p) { return __builtin_nontemporal_load(reinterpret_cast<const v4u*>(p)); }

template <int CTRL> __device__ __forceinline__ float dpp(float x) {
  return __builtin_bit_cast(float, __builtin_amdgcn_mov_dpp(__builtin_bit_cast(int, x), CTRL, 0xf, 0xf, true));
}
constexpr int XOR1 = 0xB1, XOR2 = 0x4E, XOR7 = 0x141, XOR8 = 0x128, ROR4 = 0x124, MIR = 0x140;
__device__ __forceinline__ float sw32_sum(float a, float b) {
  auto r = __builtin_amdgcn_permlane32_swap(__float_as_uint(a), __float_as_uint(b), false, false);
  return __uint_as_float(r[0]) + __uint_as_float(r[1]);
}
__device__ __forceinline__ float sw16_sum(float a, float b) {
  auto r = __builtin_amdgcn_permlane16_swap(__float_as_uint(a), __float_as_uint(b), false, false);
  return __uint_as_float(r[0]) + __uint_as_float(r[1]);
}
__device__ __forceinline__ float sw32_max(float a) {
  auto r = __builtin_amdgcn_permlane32_swap(__float_as_uint(a), __float_as_uint(a), false, false);
  return fmaxf(__uint_as_float(r[0]), __uint_as_float(r[1]));
}
__device__ __forceinline__ float sw16_max(float a) {
  auto r = __builtin_amdgcn_permlane16_swap(__float_as_uint(a), __float_as_uint(a), false, false);
  return fmaxf(__uint_as_float(r[0]), __uint_as_float(r[1]));
}
__device__ __forceinline__ float wave_sum(float v) {
  v += dpp<XOR1>(v); v += dpp<XOR2>(v); v += dpp<XOR7>(v); v += dpp<XOR8>(v);
  v = sw16_sum(v, v); v = sw32_sum(v, v);
  return v;
}
template <int H> __device__ __forceinline__ void tr32(float* v) {
#pragma unroll
  for (int j = 0; j < H; ++j) v[j] = sw32_sum(v[j], v[j + H]);
}
template <int H> __device__ __forceinline__ void tr16(float* v) {
#pragma unroll
  for (int j = 0; j < H; ++j) v[j] = sw16_sum(v[j], v[j + H]);
}
template <int H, int CTRL, int BIT> __device__ __forceinline__ void trd(float* v, int lane) {
#pragma unroll
  for (int j = 0; j < H; ++j) {
    const float t1 = v[j] + dpp<CTRL>(v[j]), t2 = v[j + H] + dpp<CTRL>(v[j + H]);
    v[j] = (lane & BIT) ? t2 : t1;
  }
}
#define BAR() do { asm volatile("s_waitcnt lgkmcnt(0)" ::: "memory"); __builtin_amdgcn_s_barrier(); asm volatile("" ::: "memory"); } while (0)
#define PIN() __builtin_amdgcn_sched_barrier(0)

__device__ __forceinline__ void fma8(float* acc, float xv, v4u w) {
#pragma unroll
  for (int k = 0; k < 4; ++k) {
    acc[2 * k] = fmaf(xv, lo(w[k]), acc[2 * k]);
    acc[2 * k + 1] = fmaf(xv, hi(w[k]), acc[2 * k + 1]);
  }
}

constexpr int L_XN = 0;
constexpr int L_QP = L_XN + 576;
constexpr int L_QB = L_QP + 576;
constexpr int L_PEX = L_QB + 128;
constexpr int L_MRG = L_PEX + 512;
constexpr int L_GUP = L_MRG + 8 * 264;
constexpr int L_OV = L_GUP + 1024;
constexpr int L_HV = L_OV + 16;
constexpr int L_FIN = L_HV + 32;
constexpr int L_RED = L_FIN + 7 * 576;
constexpr int L_END = L_RED + 160;

__device__ __forceinline__ float divr(float a, float b, float ib) { const float q = a * ib; const float r = fmaf(-q, b, a); return fmaf(r, ib, q); }
struct LnS { float mb, sd, isd; };
__device__ __forceinline__ LnS ln_stats(int lane, v4u xa, v4u xb) {
  float xv[8], xw[8];
#pragma unroll
  for (int k = 0; k < 4; ++k) { xv[2 * k] = lo(xa[k]); xv[2 * k + 1] = hi(xa[k]); xw[2 * k] = lo(xb[k]); xw[2 * k + 1] = hi(xb[k]); }
  float sm = 0.f, sm2 = 0.f;
#pragma unroll
  for (int k = 0; k < 8; ++k) { sm += xv[k]; sm2 += xw[k]; }
  sm = lane < 8 ? sm + sm2 : sm;
  const float mean = wave_sum(sm) * (1.0f / D);
  float vs = 0.f, vs2 = 0.f;
#pragma unroll
  for (int k = 0; k < 8; ++k) { const float a = xv[k] - mean; vs = fmaf(a, a, vs); const float b = xw[k] - mean; vs2 = fmaf(b, b, vs2); }
  vs = lane < 8 ? vs + vs2 : vs;
  const float var = rbf(wave_sum(vs) * (1.0f / D));
  LnS r;
  r.mb = rbf(mean);
  r.sd = rbf(sqrtf(rbf(var + 1e-5f)));
  r.isd = 1.0f / r.sd;
  return r;
}
__device__ __forceinline__ float ln_apply(float x, const LnS& s) { return rbf(divr(rbf(x - s.mb), s.sd, s.isd)); }
__device__ __forceinline__ void q_part(float* lds, int lane, int w, const LnS& s, unsigned xq0, unsigned xq1, v4u wq0, v4u wq1) {
  float acc[8] = {0, 0, 0, 0, 0, 0, 0, 0};
  fma8(acc, ln_apply(__uint_as_float(xq0 << 16), s), wq0);
  fma8(acc, ln_apply(__uint_as_float(xq1 << 16), s), wq1);
  tr32<4>(acc); tr16<2>(acc); trd<1, XOR8, 8>(acc, lane);
  lds[L_QP + 64 * w + 8 * (lane & 7) + (lane >> 3)] = acc[0];
}
__device__ __forceinline__ void ln_store8(float* dst, v4u xa, const LnS& s) {
  f32x4 o0, o1;
#pragma unroll
  for (int k = 0; k < 2; ++k) { o0[2 * k] = ln_apply(lo(xa[k]), s); o0[2 * k + 1] = ln_apply(hi(xa[k]), s); o1[2 * k] = ln_apply(lo(xa[2 + k]), s); o1[2 * k + 1] = ln_apply(hi(xa[2 + k]), s); }
  *reinterpret_cast<f32x4*>(dst) = o0;
  *reinterpret_cast<f32x4*>(dst + 4) = o1;
}

__global__ void __launch_bounds__(NTHR, 3) mega_kernel(Params p) {
  __shared__ __attribute__((aligned(16))) float lds[L_END];
  const int tid = threadIdx.x, lane = tid & 63;
  const int w = __builtin_amdgcn_readfirstlane(tid >> 6);
  const int c = blockIdx.x;
  const int h = c >> 4, s = c & 15;
  const bool heavy = (c & 31) < 27;
  const int hu = heavy ? (c >> 5) * 27 + (c & 31) : 0;
  const int li = (c >> 5) * 5 + ((c & 31) - 27);
  const __amdgpu_buffer_rsrc_t rs = __builtin_amdgcn_make_buffer_rsrc((void*)p.ws, 0, WS_END, 0x00020000);
  const int ch8 = lane & 7, rs8 = lane >> 3;
  const int r4 = lane >> 4, c16 = lane & 15;
  const int qrow0 = 144 * (s >> 2) + 16 * w;
  const u16* wqp = p.wq + (size_t)(qrow0 + rs8) * NQ + 256 * h + 64 * (s & 3) + 8 * ch8;
  STAMP(0);
  unsigned cpf = 0;
  unsigned cpt[13];

  if (w == 8) {
    __builtin_amdgcn_s_setprio(3);
    const v4u xa = *reinterpret_cast<const v4u*>(p.x + 8 * lane);
    const v4u xb = *reinterpret_cast<const v4u*>(p.x + 512 + 8 * (lane & 7));
    const unsigned xq0 = p.x[qrow0 + rs8], xq1 = p.x[qrow0 + 8 + rs8];
    const v4u wq0 = ldw(wqp), wq1 = ldw(wqp + (size_t)8 * NQ);
    const unsigned e0 = *reinterpret_cast<const unsigned*>(p.ws + WS_E);
    unsigned tlbw;
    {
      const int gr0 = 192 * (hu / 72);
      const size_t go = (size_t)gr0 * F + 64 * (hu % 72);
      const char* a = (const char*)(p.vc + ((size_t)h * SK + (SK - W) + 256 * s) * DH);
      if (lane == 1) a = (const char*)(p.wg + go);
      if (lane == 2) a = (const char*)(p.wg + go + (size_t)191 * F);
      if (lane == 3) a = (const char*)(p.wu + go);
      if (lane == 4) a = (const char*)(p.wu + go + (size_t)191 * F);
      if (lane == 5) a = (const char*)(p.wd + (size_t)18 * c * D);
      if (lane == 6) a = (const char*)(p.wo + (size_t)16 * c * D);
      if (lane == 7) a = (const char*)(p.out);
      if (lane == 8) a = (const char*)(p.ws + WS_FG + c * 4608);
      if (lane == 9) a = (const char*)(p.ws + WS_AG + c * 2304);
      if (lane == 10) a = (const char*)(p.ws + WS_HG + hu * 1024);
      if (lane == 11) a = (const char*)(p.ws + WS_FG);
      if (lane == 12) a = (const char*)(p.ws + WS_FG + 255 * 4608);
      unsigned long long pc; asm volatile("s_getpc_b64 %0" : "=s"(pc));
      if (lane >= 13 && lane < 16) a = (const char*)(pc & ~63ull) + 4096 * (lane - 12);
      tlbw = *reinterpret_cast<const unsigned*>(a);
    }
    PIN();
    __builtin_amdgcn_s_barrier();
    PIN();
    const unsigned tag0 = e0 + 1u, tag1 = ~tag0;
    const LnS lns = ln_stats(lane, xa, xb);
    STAMP(12);
    q_part(lds, lane, w, lns, xq0, xq1, wq0, wq1);
    STAMP(13);
    BAR();
    STAMP(1);
    {
      if (lane < 32) {
        float a = 0.f, b = 0.f;
#pragma unroll
        for (int ww = 0; ww < 9; ++ww) { const f32x2 v = *reinterpret_cast<const f32x2*>(&lds[L_QP + 64 * ww + 2 * lane]); a += v[0]; b += v[1]; }
        const v4u g = {__float_as_uint(a), __float_as_uint(b), tag0, tag1};
        __builtin_amdgcn_raw_buffer_store_b128(g, rs, WS_QG + c * 512 + lane * 16, 0, 16);
      }
    }
    {
      v4u g[8];
      for (unsigned spin = 0;; ++spin) {
        bool ok = true;
#pragma unroll
        for (int k = 0; k < 8; ++k) g[k] = __builtin_amdgcn_raw_buffer_load_b128(rs, WS_QG + (16 * h + (lane >> 5) + 2 * k) * 512 + (lane & 31) * 16, 0, 16);
#pragma unroll
        for (int k = 0; k < 8; ++k) ok &= (g[k][2] == tag0) & (g[k][3] == tag1);
        if (__all(ok) || spin > SPIN_CAP) break;
        asm volatile("" ::: "memory");
      }
      float a[2] = {0.f, 0.f}, b[2] = {0.f, 0.f};
#pragma unroll
      for (int k = 0; k < 8; ++k) { a[k & 1] += __uint_as_float(g[k][0]); b[k & 1] += __uint_as_float(g[k][1]); }
#pragma unroll
      for (int j = 0; j < 2; ++j) {
        const int cg = (lane >> 5) + 2 * j;
        reinterpret_cast<unsigned*>(lds)[L_QB + 32 * cg + (lane & 31)] = f2bf(a[j]) | (f2bf(b[j]) << 16);
      }
    }
    STAMP(2);
    BAR();
    BAR();
    STAMP(3);
    {
#pragma unroll
      for (int g = 0; g < 2; ++g) {
        float mw[8], M = -INFINITY;
#pragma unroll
        for (int ww = 0; ww < 8; ++ww) { mw[ww] = lds[L_MRG + (ww * 2 + g) * 132 + 128]; M = fmaxf(M, mw[ww]); }
        float L = 0.f, a = 0.f, b = 0.f;
#pragma unroll
        for (int ww = 0; ww < 8; ++ww) {
          const float wt = __expf(mw[ww] - M);
          L = fmaf(wt, lds[L_MRG + (ww * 2 + g) * 132 + 129], L);
          const f32x2 v = *reinterpret_cast<const f32x2*>(&lds[L_MRG + (ww * 2 + g) * 132 + 2 * lane]);
          a = fmaf(wt, v[0], a); b = fmaf(wt, v[1], b);
        }
        const int rec = WS_AG + c * 2304 + (g * 8 + (lane >> 3)) * 144;
        const v4u ga = {__float_as_uint(a), __float_as_uint(b), tag0, tag1};
        __builtin_amdgcn_raw_buffer_store_b128(ga, rs, rec + 16 + (lane & 7) * 16, 0, 16);
        if ((lane & 7) == 0) {
          const v4u gm = {__float_as_uint(M), __float_as_uint(L), tag0, tag1};
          __builtin_amdgcn_raw_buffer_store_b128(gm, rs, rec, 0, 16);
        }
      }
    }
    STAMP(4);
    BAR();
    STAMP(5);
    if (heavy) {
      float a = 0.f, b = 0.f;
#pragma unroll
      for (int ww = 0; ww < 8; ++ww) { a += lds[L_GUP + 128 * ww + lane]; b += lds[L_GUP + 128 * ww + 64 + lane]; }
      const v4u g = {__float_as_uint(a), __float_as_uint(b), tag0, tag1};
      __builtin_amdgcn_raw_buffer_store_b128(g, rs, WS_HG + hu * 1024 + lane * 16, 0, 16);
    }
    {
      const int pr = lane >> 2, q4 = lane & 3;
      const int rec = WS_AG + (16 * h + pr) * 2304 + s * 144;
      v4u gm, g0, g1;
      for (unsigned spin = 0;; ++spin) {
        gm = __builtin_amdgcn_raw_buffer_load_b128(rs, rec, 0, 16);
        g0 = __builtin_amdgcn_raw_buffer_load_b128(rs, rec + 16 + 32 * q4, 0, 16);
        g1 = __builtin_amdgcn_raw_buffer_load_b128(rs, rec + 32 + 32 * q4, 0, 16);
        const bool ok = (gm[2] == tag0) & (gm[3] == tag1) & (g0[2] == tag0) & (g0[3] == tag1) & (g1[2] == tag0) & (g1[3] == tag1);
        if (__all(ok) || spin > SPIN_CAP) break;
        asm volatile("" ::: "memory");
      }
      STAMP(6);
      const float m = __uint_as_float(gm[0]);
      float M = m;
#pragma unroll
      for (int k = 4; k <= 32; k <<= 1) M = fmaxf(M, __shfl_xor(M, k));
      const float wt = __expf(m - M);
      float v[5] = {wt * __uint_as_float(gm[1]), wt * __uint_as_float(g0[0]), wt * __uint_as_float(g0[1]), wt * __uint_as_float(g1[0]), wt * __uint_as_float(g1[1])};
#pragma unroll
      for (int j = 0; j < 5; ++j)
#pragma unroll
        for (int k = 4; k <= 32; k <<= 1) v[j] += __shfl_xor(v[j], k);
      if (lane < 4) {
        const float inv = 1.0f / v[0];
        f32x4 o4 = {rbf(v[1] * inv), rbf(v[2] * inv), rbf(v[3] * inv), rbf(v[4] * inv)};
        *reinterpret_cast<f32x4*>(&lds[L_OV + 4 * lane]) = o4;
      }
    }
    {
      const int ks = lane / 18, j = lane - 18 * ks;
      const int f = 18 * c + j;
      const int off = WS_HG + ((ks < 3 ? ks : 0) * 72 + (f >> 6)) * 1024 + (f & 63) * 16;
      v4u g;
      for (unsigned spin = 0;; ++spin) {
        g = __builtin_amdgcn_raw_buffer_load_b128(rs, off, 0, 16);
        const bool ok = (g[2] == tag0) & (g[3] == tag1);
        if (__all(ok) || spin > SPIN_CAP) break;
        asm volatile("" ::: "memory");
      }
      float a = __uint_as_float(g[0]), b = __uint_as_float(g[1]);
      const float a1 = __shfl(a, lane + 18), b1 = __shfl(b, lane + 18), a2 = __shfl(a, lane + 36), b2 = __shfl(b, lane + 36);
      if (lane < 18) {
        float gt = rbf(a + a1 + a2);
        const float up = rbf(b + b1 + b2);
        gt = gt > 0.f ? gt : 0.f;
        lds[L_HV + lane] = rbf(gt * up);
      }
    }
    STAMP(7);
    asm volatile("" :: "v"(cpf));
    asm volatile("" :: "v"(tlbw));
    BAR();
    return;
  }

  const size_t kvbase = ((size_t)h * SK + (SK - W) + 256 * s + 32 * w + r4) * DH + 8 * c16;
  const v4u xa = *reinterpret_cast<const v4u*>(p.x + 8 * lane);
  const v4u xb = *reinterpret_cast<const v4u*>(p.x + 512 + 8 * (lane & 7));
  const unsigned xq0 = p.x[qrow0 + rs8], xq1 = p.x[qrow0 + 8 + rs8];
  const v4u wq0 = ldw(wqp), wq1 = ldw(wqp + (size_t)8 * NQ);
  const unsigned e0 = *reinterpret_cast<const unsigned*>(p.ws + WS_E);
  PIN();
  __builtin_amdgcn_s_barrier();
  PIN();
  v4u kr[8], vr[8], gr[3], ur[3], wor[3], wdr[3];
  const int gurow0 = 192 * (hu / 72) + 24 * w;
  const size_t guo = heavy ? (size_t)(gurow0 + rs8) * F + 64 * (hu % 72) + 8 * ch8 : (size_t)0;
  const size_t gust = heavy ? (size_t)8 * F : (size_t)0;
  const int tq = tid < 504 ? tid : 503;
  const int ch72 = tq % 72, rg7 = tq / 72;
#define DEPTH 63
#define STR2(x) #x
#define STR(x) STR2(x)
#define PACE() do { PIN(); asm volatile("s_waitcnt vmcnt(" STR(DEPTH) ")" ::: "memory"); PIN(); } while (0)
#define LD_K0() _Pragma("unroll") for (int i = 0; i < 8; ++i) kr[i] = ldw(p.kc + kvbase + (size_t)(4 * i) * DH);
#define LD_GU0() _Pragma("unroll") for (int i = 0; i < 3; ++i) { gr[i] = ldw(p.wg + guo + gust * i); ur[i] = ldw(p.wu + guo + gust * i); }
#define LD_K() _Pragma("unroll") for (int i = 0; i < 8; ++i) { PACE(); kr[i] = ldw(p.kc + kvbase + (size_t)(4 * i) * DH); }
#define LD_V() _Pragma("unroll") for (int i = 0; i < 8; ++i) { PACE(); vr[i] = ldw(p.vc + kvbase + (size_t)(4 * i) * DH); }
#define LD_GU() _Pragma("unroll") for (int i = 0; i < 3; ++i) { PACE(); gr[i] = ldw(p.wg + guo + gust * i); PACE(); ur[i] = ldw(p.wu + guo + gust * i); }
#define LD_DO() _Pragma("unroll") for (int i = 0; i < 3; ++i) { const int r = min(rg7 + 7 * i, 17); PACE(); wdr[i] = ldw(p.wd + (size_t)(18 * c + r) * D + 8 * ch72); } \
                _Pragma("unroll") for (int i = 0; i < 3; ++i) { const int r = min(rg7 + 7 * i, 15); PACE(); wor[i] = ldw(p.wo + (size_t)(16 * c + r) * D + 8 * ch72); }
  LD_K0();
  PIN();
  const unsigned tag0 = e0 + 1u, tag1 = ~tag0;
  const LnS lns = ln_stats(lane, xa, xb);
  STAMP(12);
  q_part(lds, lane, w, lns, xq0, xq1, wq0, wq1);
  STAMP(13);
  BAR();
  STAMP(1);
  LD_V(); PIN(); LD_GU(); PIN(); LD_DO();
  PIN();
  if ((lane >> 3) == w) ln_store8(&lds[L_XN + 8 * lane], xa, lns);
  if (w == 7 && lane < 8) ln_store8(&lds[L_XN + 512 + 8 * lane], xb, lns);
  STAMP(2);
  BAR();
  STAMP(3);
  float pv[16];
  {
    v4u qv[2];
#pragma unroll
    for (int g = 0; g < 2; ++g) qv[g] = *reinterpret_cast<const v4u*>(&lds[L_QB + 64 * g + 4 * c16]);
    float x[16];
#pragma unroll
    for (int i = 0; i < 8; ++i)
#pragma unroll
      for (int g = 0; g < 2; ++g) {
        float d = 0.f;
#pragma unroll
        for (int k = 0; k < 4; ++k) d = dot2(kr[i][k], qv[g][k], d);
        x[2 * i + g] = d;
      }
    trd<8, XOR8, 8>(x, lane); trd<4, XOR7, 4>(x, lane); trd<2, XOR2, 2>(x, lane); trd<1, XOR1, 1>(x, lane);
    const float sc = rbf(rbf(x[0]) * 0.08838834764831845f);
    float m = sc;
    m = fmaxf(m, dpp<XOR2>(m)); m = fmaxf(m, dpp<ROR4>(m)); m = fmaxf(m, dpp<XOR8>(m)); m = sw16_max(m); m = sw32_max(m);
    const float pe = __expf(sc - m);
    float l = pe;
    l += dpp<XOR2>(l); l += dpp<ROR4>(l); l += dpp<XOR8>(l); l = sw16_sum(l, l); l = sw32_sum(l, l);
    if (lane < 2) { lds[L_MRG + (w * 2 + lane) * 132 + 128] = m; lds[L_MRG + (w * 2 + lane) * 132 + 129] = l; }
    lds[L_PEX + 64 * w + lane] = pe;
    asm volatile("s_waitcnt lgkmcnt(0)" ::: "memory");
#pragma unroll
    for (int u = 0; u < 4; ++u) {
      const f32x4 t = *reinterpret_cast<const f32x4*>(&lds[L_PEX + 64 * w + 16 * r4 + 4 * u]);
      pv[4 * u] = t[0]; pv[4 * u + 1] = t[1]; pv[4 * u + 2] = t[2]; pv[4 * u + 3] = t[3];
    }
  }
  STAMP(4);
  {
    float acc[16];
#pragma unroll
    for (int k = 0; k < 16; ++k) acc[k] = 0.f;
#pragma unroll
    for (int i = 0; i < 8; ++i) {
      fma8(acc, pv[2 * i], vr[i]);
      fma8(acc + 8, pv[2 * i + 1], vr[i]);
    }
    tr32<8>(acc); tr16<4>(acc);
    const f32x4 o4 = {acc[0], acc[1], acc[2], acc[3]};
    *reinterpret_cast<f32x4*>(&lds[L_MRG + (w * 2 + (lane >> 5)) * 132 + 8 * c16 + 4 * ((lane >> 4) & 1)]) = o4;
  }
  STAMP(5);
  BAR();
  {
    float ag[8] = {0, 0, 0, 0, 0, 0, 0, 0}, au[8] = {0, 0, 0, 0, 0, 0, 0, 0};
#pragma unroll
    for (int i = 0; i < 3; ++i) {
      const float xv = lds[L_XN + gurow0 + 8 * i + rs8];
      fma8(ag, xv, gr[i]);
      fma8(au, xv, ur[i]);
    }
    tr32<4>(ag); tr16<2>(ag); trd<1, XOR8, 8>(ag, lane);
    tr32<4>(au); tr16<2>(au); trd<1, XOR8, 8>(au, lane);
    lds[L_GUP + 128 * w + 8 * ch8 + rs8] = ag[0];
    lds[L_GUP + 128 * w + 64 + 8 * ch8 + rs8] = au[0];
  }
  STAMP(6);
  BAR();
  BAR();
  STAMP(7);
  if (tid < 504) {
    float acc[8] = {0, 0, 0, 0, 0, 0, 0, 0};
#pragma unroll
    for (int i = 0; i < 3; ++i) {
      const int r = rg7 + 7 * i;
      const float hv = r < 18 ? lds[L_HV + min(r, 17)] : 0.f, ov = r < 16 ? lds[L_OV + min(r, 15)] : 0.f;
      fma8(acc, hv, wdr[i]);
      fma8(acc, ov, wor[i]);
    }
    const f32x4 a0 = {acc[0], acc[1], acc[2], acc[3]}, a1 = {acc[4], acc[5], acc[6], acc[7]};
    *reinterpret_cast<f32x4*>(&lds[L_FIN + 576 * rg7 + 8 * ch72]) = a0;
    *reinterpret_cast<f32x4*>(&lds[L_FIN + 576 * rg7 + 8 * ch72 + 4]) = a1;
  }
  BAR();
  if (tid < 288) {
    float a = 0.f, b = 0.f;
#pragma unroll
    for (int r = 0; r < 7; ++r) { const f32x2 v = *reinterpret_cast<const f32x2*>(&lds[L_FIN + 576 * r + 2 * tid]); a += v[0]; b += v[1]; }
    const v4u g = {__float_as_uint(a), __float_as_uint(b), tag0, tag1};
    __builtin_amdgcn_raw_buffer_store_b128(g, rs, WS_FG + c * 4608 + tid * 16, 0, 16);
  }
  STAMP(8);
  if (heavy || li >= 36) return;
  {
    const int gi = tid & 7, p0 = tid >> 3;
    v4u g[4];
    for (unsigned spin = 0;; ++spin) {
      bool ok = true;
#pragma unroll
      for (int k = 0; k < 4; ++k) g[k] = __builtin_amdgcn_raw_buffer_load_b128(rs, WS_FG + (p0 + 64 * k) * 4608 + li * 128 + gi * 16, 0, 16);
#pragma unroll
      for (int k = 0; k < 4; ++k) ok &= (g[k][2] == tag0) & (g[k][3] == tag1);
      if (__all(ok) || spin > SPIN_CAP) break;
      asm volatile("" ::: "memory");
    }
    STAMP(9);
    float a = 0.f, b = 0.f;
#pragma unroll
    for (int k = 0; k < 4; ++k) { a += __uint_as_float(g[k][0]); b += __uint_as_float(g[k][1]); }
#pragma unroll
    for (int k = 8; k <= 32; k <<= 1) { a += __shfl_xor(a, k); b += __shfl_xor(b, k); }
    if (lane < 8) { lds[L_RED + 16 * w + 2 * lane] = a; lds[L_RED + 16 * w + 2 * lane + 1] = b; }
    BAR();
    if (tid < 16) {
      float t = 0.f;
#pragma unroll
      for (int ww = 0; ww < 8; ++ww) t += lds[L_RED + 16 * ww + tid];
      const int n = 16 * li + tid;
      const float xv = __uint_as_float((unsigned)p.x[n] << 16);
      p.out[n] = (u16)f2bf(xv + t);
    }
    if (li == 0 && tid == 0) *reinterpret_cast<unsigned*>(p.ws + WS_E) = tag0;
    STAMP(10);
  }
}

extern "C" void kernel_launch(void* const* d_in, const int* in_sizes, int n_in, void* d_out, int out_size, void* d_ws, size_t ws_size,
                              hipStream_t stream) {
  Params p;
  p.x = (const u16*)d_in[0];
  p.kc = (const u16*)d_in[1];
  p.vc = (const u16*)d_in[2];
  p.wq = (const u16*)d_in[3];
  p.wo = (const u16*)d_in[4];
  p.wg = (const u16*)d_in[6];
  p.wu = (const u16*)d_in[7];
  p.wd = (const u16*)d_in[8];
  p.out = (u16*)d_out;
  p.ws = (unsigned char*)d_ws;
  mega_kernel<<<256, NTHR, 0, stream>>>(p);
}
```

```cpp
#include <hip/hip_runtime.h>
#include <hip/hip_bf16.h>
#include <stdint.h>

typedef unsigned v4u __attribute__((ext_vector_type(4)));
typedef float f32x4 __attribute__((ext_vector_type(4)));
typedef float f32x2 __attribute__((ext_vector_type(2)));
typedef unsigned short u16;
using bf16x2 = __attribute__((ext_vector_type(2))) __bf16;

constexpr int D = 576, SK = 32768, DH = 128, W = 4096, F = 4608, NQ = 4096;
constexpr int NTHR = 576;
#define ORDER 0
constexpr int WS_E = 0;
constexpr int WS_QG = 4096;
constexpr int WS_AG = WS_QG + 256 * 512;
constexpr int WS_HG = WS_AG + 256 * 2304;
constexpr int WS_FG = WS_HG + 216 * 1024;
constexpr int WS_END = WS_FG + 256 * 4608;
constexpr int WS_STAMP = 4 << 20;
constexpr unsigned SPIN_CAP = 1u << 16;

#define STAMP(i) do {} while (0)

struct Params {
  const u16 *x, *kc, *vc, *wq, *wo, *wg, *wu, *wd;
  u16* out;
  unsigned char* ws;
};

__device__ __forceinline__ float rbf(float f) {
  unsigned u = __float_as_uint(f);
  u += 0x7FFFu + ((u >> 16) & 1u);
  return __uint_as_float(u & 0xFFFF0000u);
}
__device__ __forceinline__ unsigned f2bf(float f) {
  unsigned u = __float_as_uint(f);
  u += 0x7FFFu + ((u >> 16) & 1u);
  return u >> 16;
}
__device__ __forceinline__ float lo(unsigned w) { return __uint_as_float(w << 16); }
__device__ __forceinline__ float hi(unsigned w) { return __uint_as_float(w & 0xFFFF0000u); }
__device__ __forceinline__ float dot2(unsigned k, unsigned q, float acc) {
  return __builtin_amdgcn_fdot2_f32_bf16(__builtin_bit_cast(bf16x2, k), __builtin_bit_cast(bf16x2, q), acc, false);
}
__device__ __forceinline__ v4u ldw(const u16* p) { return __builtin_nontemporal_load(reinterpret_cast<const v4u*>(p)); }

template <int CTRL> __device__ __forceinline__ float dpp(float x) {
  return __builtin_bit_cast(float, __builtin_amdgcn_mov_dpp(__builtin_bit_cast(int, x), CTRL, 0xf, 0xf, true));
}
constexpr int XOR1 = 0xB1, XOR2 = 0x4E, XOR7 = 0x141, XOR8 = 0x128, ROR4 = 0x124, MIR = 0x140;
__device__ __forceinline__ float sw32_sum(float a, float b) {
  auto r = __builtin_amdgcn_permlane32_swap(__float_as_uint(a), __float_as_uint(b), false, false);
  return __uint_as_float(r[0]) + __uint_as_float(r[1]);
}
__device__ __forceinline__ float sw16_sum(float a, float b) {
  auto r = __builtin_amdgcn_permlane16_swap(__float_as_uint(a), __float_as_uint(b), false, false);
  return __uint_as_float(r[0]) + __uint_as_float(r[1]);
}
__device__ __forceinline__ float sw32_max(float a) {
  auto r = __builtin_amdgcn_permlane32_swap(__float_as_uint(a), __float_as_uint(a), false, false);
  return fmaxf(__uint_as_float(r[0]), __uint_as_float(r[1]));
}
__device__ __forceinline__ float sw16_max(float a) {
  auto r = __builtin_amdgcn_permlane16_swap(__float_as_uint(a), __float_as_uint(a), false, false);
  return fmaxf(__uint_as_float(r[0]), __uint_as_float(r[1]));
}
__device__ __forceinline__ float wave_sum(float v) {
  v += dpp<XOR1>(v); v += dpp<XOR2>(v); v += dpp<XOR7>(v); v += dpp<XOR8>(v);
  v = sw16_sum(v, v); v = sw32_sum(v, v);
  return v;
}
template <int H> __device__ __forceinline__ void tr32(float* v) {
#pragma unroll
  for (int j = 0; j < H; ++j) v[j] = sw32_sum(v[j], v[j + H]);
}
template <int H> __device__ __forceinline__ void tr16(float* v) {
#pragma unroll
  for (int j = 0; j < H; ++j) v[j] = sw16_sum(v[j], v[j + H]);
}
template <int H, int CTRL, int BIT> __device__ __forceinline__ void trd(float* v, int lane) {
#pragma unroll
  for (int j = 0; j < H; ++j) {
    const float t1 = v[j] + dpp<CTRL>(v[j]), t2 = v[j + H] + dpp<CTRL>(v[j + H]);
    v[j] = (lane & BIT) ? t2 : t1;
  }
}
#define BAR() do { __builtin_amdgcn_sched_barrier(0); asm volatile("s_waitcnt lgkmcnt(0)" ::: "memory"); __builtin_amdgcn_s_barrier(); asm volatile("" ::: "memory"); __builtin_amdgcn_sched_barrier(0); } while (0)
#define PIN() __builtin_amdgcn_sched_barrier(0)

__device__ __forceinline__ void fma8(float* acc, float xv, v4u w) {
#pragma unroll
  for (int k = 0; k < 4; ++k) {
    acc[2 * k] = fmaf(xv, lo(w[k]), acc[2 * k]);
    acc[2 * k + 1] = fmaf(xv, hi(w[k]), acc[2 * k + 1]);
  }
}

constexpr int L_XN = 0;
constexpr int L_QP = L_XN + 576;
constexpr int L_QB = L_QP + 576;
constexpr int L_PEX = L_QB + 128;
constexpr int L_MRG = L_PEX + 512;
constexpr int L_GUP = L_MRG + 8 * 264;
constexpr int L_OV = L_GUP + 1024;
constexpr int L_HV = L_OV + 16;
constexpr int L_FIN = L_HV + 32;
constexpr int L_RED = L_FIN + 7 * 576;
constexpr int L_CNT = L_RED + 160;
constexpr int L_END = L_CNT + 4;

__device__ __forceinline__ float divr(float a, float b, float ib) { const float q = a * ib; const float r = fmaf(-q, b, a); return fmaf(r, ib, q); }
struct LnS { float mb, sd, isd; };
__device__ __forceinline__ LnS ln_stats(int lane, v4u xa, v4u xb) {
  float xv[8], xw[8];
#pragma unroll
  for (int k = 0; k < 4; ++k) { xv[2 * k] = lo(xa[k]); xv[2 * k + 1] = hi(xa[k]); xw[2 * k] = lo(xb[k]); xw[2 * k + 1] = hi(xb[k]); }
  float sm = 0.f, sm2 = 0.f;
#pragma unroll
  for (int k = 0; k < 8; ++k) { sm += xv[k]; sm2 += xw[k]; }
  sm = lane < 8 ? sm + sm2 : sm;
  const float mean = wave_sum(sm) * (1.0f / D);
  float vs = 0.f, vs2 = 0.f;
#pragma unroll
  for (int k = 0; k < 8; ++k) { const float a = xv[k] - mean; vs = fmaf(a, a, vs); const float b = xw[k] - mean; vs2 = fmaf(b, b, vs2); }
  vs = lane < 8 ? vs + vs2 : vs;
  const float var = rbf(wave_sum(vs) * (1.0f / D));
  LnS r;
  r.mb = rbf(mean);
  r.sd = rbf(sqrtf(rbf(var + 1e-5f)));
  r.isd = 1.0f / r.sd;
  return r;
}
__device__ __forceinline__ float ln_apply(float x, const LnS& s) { return rbf(divr(rbf(x - s.mb), s.sd, s.isd)); }
__device__ __forceinline__ void q_part(float* lds, int lane, int w, const LnS& s, unsigned xq0, unsigned xq1, v4u wq0, v4u wq1) {
  float acc[8] = {0, 0, 0, 0, 0, 0, 0, 0};
  fma8(acc, ln_apply(__uint_as_float(xq0 << 16), s), wq0);
  fma8(acc, ln_apply(__uint_as_float(xq1 << 16), s), wq1);
  tr32<4>(acc); tr16<2>(acc); trd<1, XOR8, 8>(acc, lane);
  lds[L_QP + 64 * w + 8 * (lane & 7) + (lane >> 3)] = acc[0];
}
__device__ __forceinline__ void ln_store8(float* dst, v4u xa, const LnS& s) {
  f32x4 o0, o1;
#pragma unroll
  for (int k = 0; k < 2; ++k) { o0[2 * k] = ln_apply(lo(xa[k]), s); o0[2 * k + 1] = ln_apply(hi(xa[k]), s); o1[2 * k] = ln_apply(lo(xa[2 + k]), s); o1[2 * k + 1] = ln_apply(hi(xa[2 + k]), s); }
  *reinterpret_cast<f32x4*>(dst) = o0;
  *reinterpret_cast<f32x4*>(dst + 4) = o1;
}

__global__ void __launch_bounds__(NTHR, 3) mega_kernel(Params p) {
  __shared__ __attribute__((aligned(16))) float lds[L_END];
  const int tid = threadIdx.x, lane = tid & 63;
  const int w = __builtin_amdgcn_readfirstlane(tid >> 6);
  const int c = blockIdx.x;
  const int h = c >> 4, s = c & 15;
  const bool heavy = (c & 31) < 27;
  const int hu = heavy ? (c >> 5) * 27 + (c & 31) : 0;
  const int li = (c >> 5) * 5 + ((c & 31) - 27);
  const __amdgpu_buffer_rsrc_t rs = __builtin_amdgcn_make_buffer_rsrc((void*)p.ws, 0, WS_END, 0x00020000);
  const int ch8 = lane & 7, rs8 = lane >> 3;
  const int r4 = lane >> 4, c16 = lane & 15;
  const int qrow0 = 144 * (s >> 2) + 16 * w;
  const u16* wqp = p.wq + (size_t)(qrow0 + rs8) * NQ + 256 * h + 64 * (s & 3) + 8 * ch8;
  STAMP(0);

  if (w == 8) {
    __builtin_amdgcn_s_setprio(3);
    const v4u xa = *reinterpret_cast<const v4u*>(p.x + 8 * lane);
    const v4u xb = *reinterpret_cast<const v4u*>(p.x + 512 + 8 * (lane & 7));
    const unsigned xq0 = p.x[qrow0 + rs8], xq1 = p.x[qrow0 + 8 + rs8];
    const v4u wq0 = ldw(wqp), wq1 = ldw(wqp + (size_t)8 * NQ);
    const unsigned e0 = *reinterpret_cast<const unsigned*>(p.ws + WS_E);
    if (lane == 0) reinterpret_cast<unsigned*>(lds)[L_CNT] = 0u;
    PIN();
    BAR();
    PIN();
    const unsigned tag0 = e0 + 1u, tag1 = ~tag0;
    const LnS lns = ln_stats(lane, xa, xb);
    STAMP(12);
    q_part(lds, lane, w, lns, xq0, xq1, wq0, wq1);
    STAMP(13);
    BAR();
    STAMP(1);
    {
      if (lane < 32) {
        float a = 0.f, b = 0.f;
#pragma unroll
        for (int ww = 0; ww < 9; ++ww) { const f32x2 v = *reinterpret_cast<const f32x2*>(&lds[L_QP + 64 * ww + 2 * lane]); a += v[0]; b += v[1]; }
        const v4u g = {__float_as_uint(a), __float_as_uint(b), tag0, tag1};
        __builtin_amdgcn_raw_buffer_store_b128(g, rs, WS_QG + c * 512 + lane * 16, 0, 16);
      }
    }
    {
      v4u g[8];
      for (unsigned spin = 0;; ++spin) {
        bool ok = true;
#pragma unroll
        for (int k = 0; k < 8; ++k) g[k] = __builtin_amdgcn_raw_buffer_load_b128(rs, WS_QG + (16 * h + (lane >> 5) + 2 * k) * 512 + (lane & 31) * 16, 0, 16);
#pragma unroll
        for (int k = 0; k < 8; ++k) ok &= (g[k][2] == tag0) & (g[k][3] == tag1);
        if (__all(ok) || spin > SPIN_CAP) break;
        asm volatile("" ::: "memory");
      }
      float a[2] = {0.f, 0.f}, b[2] = {0.f, 0.f};
#pragma unroll
      for (int k = 0; k < 8; ++k) { a[k & 1] += __uint_as_float(g[k][0]); b[k & 1] += __uint_as_float(g[k][1]); }
#pragma unroll
      for (int j = 0; j < 2; ++j) {
        const int cg = (lane >> 5) + 2 * j;
        reinterpret_cast<unsigned*>(lds)[L_QB + 32 * cg + (lane & 31)] = f2bf(a[j]) | (f2bf(b[j]) << 16);
      }
    }
    STAMP(2);
    BAR();
    {
      const int ks = lane / 18, j = lane - 18 * ks;
      const int f = 18 * c + j;
      const int off = WS_HG + ((ks < 3 ? ks : 0) * 72 + (f >> 6)) * 1024 + (f & 63) * 16;
      v4u g;
      for (unsigned spin = 0;; ++spin) {
        g = __builtin_amdgcn_raw_buffer_load_b128(rs, off, 0, 16);
        const bool ok = (g[2] == tag0) & (g[3] == tag1);
        if (__all(ok) || spin > SPIN_CAP) break;
        asm volatile("" ::: "memory");
      }
      float a = __uint_as_float(g[0]), b = __uint_as_float(g[1]);
      const float a1 = __shfl(a, lane + 18), b1 = __shfl(b, lane + 18), a2 = __shfl(a, lane + 36), b2 = __shfl(b, lane + 36);
      if (lane < 18) {
        float gt = rbf(a + a1 + a2);
        const float up = rbf(b + b1 + b2);
        gt = gt > 0.f ? gt : 0.f;
        lds[L_HV + lane] = rbf(gt * up);
      }
    }
    STAMP(5);
    BAR();
    STAMP(3);
    {
#pragma unroll
      for (int g = 0; g < 2; ++g) {
        float mw[8], M = -INFINITY;
#pragma unroll
        for (int ww = 0; ww < 8; ++ww) { mw[ww] = lds[L_MRG + (ww * 2 + g) * 132 + 128]; M = fmaxf(M, mw[ww]); }
        float L = 0.f, a = 0.f, b = 0.f;
#pragma unroll
        for (int ww = 0; ww < 8; ++ww) {
          const float wt = __expf(mw[ww] - M);
          L = fmaf(wt, lds[L_MRG + (ww * 2 + g) * 132 + 129], L);
          const f32x2 v = *reinterpret_cast<const f32x2*>(&lds[L_MRG + (ww * 2 + g) * 132 + 2 * lane]);
          a = fmaf(wt, v[0], a); b = fmaf(wt, v[1], b);
        }
        const int rec = WS_AG + c * 2304 + (g * 8 + (lane >> 3)) * 144;
        const v4u ga = {__float_as_uint(a), __float_as_uint(b), tag0, tag1};
        __builtin_amdgcn_raw_buffer_store_b128(ga, rs, rec + 16 + (lane & 7) * 16, 0, 16);
        if ((lane & 7) == 0) {
          const v4u gm = {__float_as_uint(M), __float_as_uint(L), tag0, tag1};
          __builtin_amdgcn_raw_buffer_store_b128(gm, rs, rec, 0, 16);
        }
      }
    }
    STAMP(4);
    {
      const int pr = lane >> 2, q4 = lane & 3;
      const int rec = WS_AG + (16 * h + pr) * 2304 + s * 144;
      v4u gm, g0, g1;
      for (unsigned spin = 0;; ++spin) {
        gm = __builtin_amdgcn_raw_buffer_load_b128(rs, rec, 0, 16);
        g0 = __builtin_amdgcn_raw_buffer_load_b128(rs, rec + 16 + 32 * q4, 0, 16);
        g1 = __builtin_amdgcn_raw_buffer_load_b128(rs, rec + 32 + 32 * q4, 0, 16);
        const bool ok = (gm[2] == tag0) & (gm[3] == tag1) & (g0[2] == tag0) & (g0[3] == tag1) & (g1[2] == tag0) & (g1[3] == tag1);
        if (__all(ok) || spin > SPIN_CAP) break;
        asm volatile("" ::: "memory");
      }
      STAMP(6);
      const float m = __uint_as_float(gm[0]);
      float M = m;
#pragma unroll
      for (int k = 4; k <= 32; k <<= 1) M = fmaxf(M, __shfl_xor(M, k));
      const float wt = __expf(m - M);
      float v[5] = {wt * __uint_as_float(gm[1]), wt * __uint_as_float(g0[0]), wt * __uint_as_float(g0[1]), wt * __uint_as_float(g1[0]), wt * __uint_as_float(g1[1])};
#pragma unroll
      for (int j = 0; j < 5; ++j)
#pragma unroll
        for (int k = 4; k <= 32; k <<= 1) v[j] += __shfl_xor(v[j], k);
      if (lane < 4) {
        const float inv = 1.0f / v[0];
        f32x4 o4 = {rbf(v[1] * inv), rbf(v[2] * inv), rbf(v[3] * inv), rbf(v[4] * inv)};
        *reinterpret_cast<f32x4*>(&lds[L_OV + 4 * lane]) = o4;
      }
    }
    STAMP(7);
    BAR();
    return;
  }

  const size_t kvbase = ((size_t)h * SK + (SK - W) + 256 * s + 32 * w + r4) * DH + 8 * c16;
  const v4u xa = *reinterpret_cast<const v4u*>(p.x + 8 * lane);
  const v4u xb = *reinterpret_cast<const v4u*>(p.x + 512 + 8 * (lane & 7));
  const int gurow0 = 192 * (hu / 72) + 24 * w;
  const unsigned xq0 = p.x[qrow0 + rs8], xq1 = p.x[qrow0 + 8 + rs8];
  const unsigned xg0 = p.x[gurow0 + rs8], xg1 = p.x[gurow0 + 8 + rs8], xg2 = p.x[gurow0 + 16 + rs8];
  const v4u wq0 = ldw(wqp), wq1 = ldw(wqp + (size_t)8 * NQ);
  const unsigned e0 = *reinterpret_cast<const unsigned*>(p.ws + WS_E);
  PIN();
  BAR();
  PIN();
  v4u kr[8], vr[8], gr[3], ur[3], wor[3], wdr[3];
  const size_t guo = heavy ? (size_t)(gurow0 + rs8) * F + 64 * (hu % 72) + 8 * ch8 : (size_t)0;
  const size_t gust = heavy ? (size_t)8 * F : (size_t)0;
  const int tq = tid < 504 ? tid : 503;
  const int ch72 = tq % 72, rg7 = tq / 72;
#define DEPTH 63
#define STR2(x) #x
#define STR(x) STR2(x)
#define PACE() do { PIN(); asm volatile("s_waitcnt vmcnt(" STR(DEPTH) ")" ::: "memory"); PIN(); } while (0)
#define LD_K0() _Pragma("unroll") for (int i = 0; i < 8; ++i) kr[i] = ldw(p.kc + kvbase + (size_t)(4 * i) * DH);
#define LD_GU0() _Pragma("unroll") for (int i = 0; i < 3; ++i) { gr[i] = ldw(p.wg + guo + gust * i); ur[i] = ldw(p.wu + guo + gust * i); }
#define LD_K() _Pragma("unroll") for (int i = 0; i < 8; ++i) { PACE(); kr[i] = ldw(p.kc + kvbase + (size_t)(4 * i) * DH); }
#define LD_V() _Pragma("unroll") for (int i = 0; i < 8; ++i) { PACE(); vr[i] = ldw(p.vc + kvbase + (size_t)(4 * i) * DH); }
#define LD_GU() _Pragma("unroll") for (int i = 0; i < 3; ++i) { PACE(); gr[i] = ldw(p.wg + guo + gust * i); PACE(); ur[i] = ldw(p.wu + guo + gust * i); }
#define LD_DO() _Pragma("unroll") for (int i = 0; i < 3; ++i) { const int r = min(rg7 + 7 * i, 17); PACE(); wdr[i] = ldw(p.wd + (size_t)(18 * c + r) * D + 8 * ch72); } \
                _Pragma("unroll") for (int i = 0; i < 3; ++i) { const int r = min(rg7 + 7 * i, 15); PACE(); wor[i] = ldw(p.wo + (size_t)(16 * c + r) * D + 8 * ch72); }
  LD_GU0();
  PIN();
  const unsigned tag0 = e0 + 1u, tag1 = ~tag0;
  const LnS lns = ln_stats(lane, xa, xb);
  STAMP(12);
  q_part(lds, lane, w, lns, xq0, xq1, wq0, wq1);
  STAMP(13);
  BAR();
  STAMP(1);
  LD_K0();
  PIN();
  {
    float ag[8] = {0, 0, 0, 0, 0, 0, 0, 0}, au[8] = {0, 0, 0, 0, 0, 0, 0, 0};
    const float xv[3] = {ln_apply(__uint_as_float(xg0 << 16), lns), ln_apply(__uint_as_float(xg1 << 16), lns), ln_apply(__uint_as_float(xg2 << 16), lns)};
#pragma unroll
    for (int i = 0; i < 3; ++i) {
      fma8(ag, xv[i], gr[i]);
      fma8(au, xv[i], ur[i]);
    }
    tr32<4>(ag); tr16<2>(ag); trd<1, XOR8, 8>(ag, lane);
    tr32<4>(au); tr16<2>(au); trd<1, XOR8, 8>(au, lane);
    lds[L_GUP + 128 * w + 8 * ch8 + rs8] = ag[0];
    lds[L_GUP + 128 * w + 64 + 8 * ch8 + rs8] = au[0];
    asm volatile("s_waitcnt lgkmcnt(0)" ::: "memory");
    unsigned old = 0;
    if (lane == 0) old = __hip_atomic_fetch_add(reinterpret_cast<unsigned*>(lds) + L_CNT, 1u, __ATOMIC_RELAXED, __HIP_MEMORY_SCOPE_WORKGROUP);
    old = __builtin_amdgcn_readfirstlane(old);
    if (old == 7u && heavy) {
      asm volatile("" ::: "memory");
      float a = 0.f, b = 0.f;
#pragma unroll
      for (int ww = 0; ww < 8; ++ww) { a += lds[L_GUP + 128 * ww + lane]; b += lds[L_GUP + 128 * ww + 64 + lane]; }
      const v4u g = {__float_as_uint(a), __float_as_uint(b), tag0, tag1};
      __builtin_amdgcn_raw_buffer_store_b128(g, rs, WS_HG + hu * 1024 + lane * 16, 0, 16);
    }
  }
  STAMP(6);
  LD_V(); PIN(); LD_DO();
  PIN();
  STAMP(2);
  BAR();
  STAMP(3);
  float pv[16];
  {
    v4u qv[2];
#pragma unroll
    for (int g = 0; g < 2; ++g) qv[g] = *reinterpret_cast<const v4u*>(&lds[L_QB + 64 * g + 4 * c16]);
    float x[16];
#pragma unroll
    for (int i = 0; i < 8; ++i)
#pragma unroll
      for (int g = 0; g < 2; ++g) {
        float d = 0.f;
#pragma unroll
        for (int k = 0; k < 4; ++k) d = dot2(kr[i][k], qv[g][k], d);
        x[2 * i + g] = d;
      }
    trd<8, XOR8, 8>(x, lane); trd<4, XOR7, 4>(x, lane); trd<2, XOR2, 2>(x, lane); trd<1, XOR1, 1>(x, lane);
    const float sc = rbf(rbf(x[0]) * 0.08838834764831845f);
    float m = sc;
    m = fmaxf(m, dpp<XOR2>(m)); m = fmaxf(m, dpp<ROR4>(m)); m = fmaxf(m, dpp<XOR8>(m)); m = sw16_max(m); m = sw32_max(m);
    const float pe = __expf(sc - m);
    float l = pe;
    l += dpp<XOR2>(l); l += dpp<ROR4>(l); l += dpp<XOR8>(l); l = sw16_sum(l, l); l = sw32_sum(l, l);
    if (lane < 2) { lds[L_MRG + (w * 2 + lane) * 132 + 128] = m; lds[L_MRG + (w * 2 + lane) * 132 + 129] = l; }
    lds[L_PEX + 64 * w + lane] = pe;
    asm volatile("s_waitcnt lgkmcnt(0)" ::: "memory");
#pragma unroll
    for (int u = 0; u < 4; ++u) {
      const f32x4 t = *reinterpret_cast<const f32x4*>(&lds[L_PEX + 64 * w + 16 * r4 + 4 * u]);
      pv[4 * u] = t[0]; pv[4 * u + 1] = t[1]; pv[4 * u + 2] = t[2]; pv[4 * u + 3] = t[3];
    }
  }
  STAMP(4);
  {
    float acc[16];
#pragma unroll
    for (int k = 0; k < 16; ++k) acc[k] = 0.f;
#pragma unroll
    for (int i = 0; i < 8; ++i) {
      fma8(acc, pv[2 * i], vr[i]);
      fma8(acc + 8, pv[2 * i + 1], vr[i]);
    }
    tr32<8>(acc); tr16<4>(acc);
    const f32x4 o4 = {acc[0], acc[1], acc[2], acc[3]};
    *reinterpret_cast<f32x4*>(&lds[L_MRG + (w * 2 + (lane >> 5)) * 132 + 8 * c16 + 4 * ((lane >> 4) & 1)]) = o4;
  }
  STAMP(5);
  BAR();
  BAR();
  STAMP(7);
  if (tid < 504) {
    float acc[8] = {0, 0, 0, 0, 0, 0, 0, 0};
#pragma unroll
    for (int i = 0; i < 3; ++i) {
      const int r = rg7 + 7 * i;
      const float hv = r < 18 ? lds[L_HV + min(r, 17)] : 0.f, ov = r < 16 ? lds[L_OV + min(r, 15)] : 0.f;
      fma8(acc, hv, wdr[i]);
      fma8(acc, ov, wor[i]);
    }
    const f32x4 a0 = {acc[0], acc[1], acc[2], acc[3]}, a1 = {acc[4], acc[5], acc[6], acc[7]};
    *reinterpret_cast<f32x4*>(&lds[L_FIN + 576 * rg7 + 8 * ch72]) = a0;
    *reinterpret_cast<f32x4*>(&lds[L_FIN + 576 * rg7 + 8 * ch72 + 4]) = a1;
  }
  BAR();
  if (tid < 288) {
    float a = 0.f, b = 0.f;
#pragma unroll
    for (int r = 0; r < 7; ++r) { const f32x2 v = *reinterpret_cast<const f32x2*>(&lds[L_FIN + 576 * r + 2 * tid]); a += v[0]; b += v[1]; }
    const v4u g = {__float_as_uint(a), __float_as_uint(b), tag0, tag1};
    __builtin_amdgcn_raw_buffer_store_b128(g, rs, WS_FG + c * 4608 + tid * 16, 0, 16);
  }
  STAMP(8);
  if (heavy || li >= 36) return;
  {
    const int gi = tid & 7, p0 = tid >> 3;
    v4u g[4];
    for (unsigned spin = 0;; ++spin) {
      bool ok = true;
#pragma unroll
      for (int k = 0; k < 4; ++k) g[k] = __builtin_amdgcn_raw_buffer_load_b128(rs, WS_FG + (p0 + 64 * k) * 4608 + li * 128 + gi * 16, 0, 16);
#pragma unroll
      for (int k = 0; k < 4; ++k) ok &= (g[k][2] == tag0) & (g[k][3] == tag1);
      if (__all(ok) || spin > SPIN_CAP) break;
      asm volatile("" ::: "memory");
    }
    STAMP(9);
    float a = 0.f, b = 0.f;
#pragma unroll
    for (int k = 0; k < 4; ++k) { a += __uint_as_float(g[k][0]); b += __uint_as_float(g[k][1]); }
#pragma unroll
    for (int k = 8; k <= 32; k <<= 1) { a += __shfl_xor(a, k); b += __shfl_xor(b, k); }
    if (lane < 8) { lds[L_RED + 16 * w + 2 * lane] = a; lds[L_RED + 16 * w + 2 * lane + 1] = b; }
    BAR();
    if (tid < 16) {
      float t = 0.f;
#pragma unroll
      for (int ww = 0; ww < 8; ++ww) t += lds[L_RED + 16 * ww + tid];
      const int n = 16 * li + tid;
      const float xv = __uint_as_float((unsigned)p.x[n] << 16);
      p.out[n] = (u16)f2bf(xv + t);
    }
    if (li == 0 && tid == 0) *reinterpret_cast<unsigned*>(p.ws + WS_E) = tag0;
    STAMP(10);
  }
}

extern "C" void kernel_launch(void* const* d_in, const int* in_sizes, int n_in, void* d_out, int out_size, void* d_ws, size_t ws_size,
                              hipStream_t stream) {
  Params p;
  p.x = (const u16*)d_in[0];
  p.kc = (const u16*)d_in[1];
  p.vc = (const u16*)d_in[2];
  p.wq = (const u16*)d_in[3];
  p.wo = (const u16*)d_in[4];
  p.wg = (const u16*)d_in[6];
  p.wu = (const u16*)d_in[7];
  p.wd = (const u16*)d_in[8];
  p.out = (u16*)d_out;
  p.ws = (unsigned char*)d_ws;
  mega_kernel<<<256, NTHR, 0, stream>>>(p);
}
```

```cpp
#include <hip/hip_runtime.h>
#include <hip/hip_bf16.h>
#include <stdint.h>

typedef unsigned v4u __attribute__((ext_vector_type(4)));
typedef float f32x4 __attribute__((ext_vector_type(4)));
typedef float f32x2 __attribute__((ext_vector_type(2)));
typedef unsigned short u16;
using bf16x2 = __attribute__((ext_vector_type(2))) __bf16;

constexpr int D = 576, SK = 32768, DH = 128, W = 4096, F = 4608, NQ = 4096;
constexpr int NTHR = 576;
#define GUFIRST 1
#define ORDER 0
constexpr int WS_E = 0;
constexpr int WS_QG = 4096;
constexpr int WS_AG = WS_QG + 256 * 512;
constexpr int WS_HG = WS_AG + 256 * 2304;
constexpr int WS_FG = WS_HG + 216 * 1024;
constexpr int WS_END = WS_FG + 256 * 4608;
constexpr int WS_STAMP = 4 << 20;
constexpr unsigned SPIN_CAP = 1u << 16;

#define STAMP(i) do {} while (0)

struct Params {
  const u16 *x, *kc, *vc, *wq, *wo, *wg, *wu, *wd;
  u16* out;
  unsigned char* ws;
};

__device__ __forceinline__ float rbf(float f) {
  unsigned u = __float_as_uint(f);
  u += 0x7FFFu + ((u >> 16) & 1u);
  return __uint_as_float(u & 0xFFFF0000u);
}
__device__ __forceinline__ unsigned f2bf(float f) {
  unsigned u = __float_as_uint(f);
  u += 0x7FFFu + ((u >> 16) & 1u);
  return u >> 16;
}
__device__ __forceinline__ float lo(unsigned w) { return __uint_as_float(w << 16); }
__device__ __forceinline__ float hi(unsigned w) { return __uint_as_float(w & 0xFFFF0000u); }
__device__ __forceinline__ float dot2(unsigned k, unsigned q, float acc) {
  return __builtin_amdgcn_fdot2_f32_bf16(__builtin_bit_cast(bf16x2, k), __builtin_bit_cast(bf16x2, q), acc, false);
}
__device__ __forceinline__ v4u ldw(const u16* p) { return *reinterpret_cast<const v4u*>(p); }

template <int CTRL> __device__ __forceinline__ float dpp(float x) {
  return __builtin_bit_cast(float, __builtin_amdgcn_mov_dpp(__builtin_bit_cast(int, x), CTRL, 0xf, 0xf, true));
}
constexpr int XOR1 = 0xB1, XOR2 = 0x4E, XOR7 = 0x141, XOR8 = 0x128, ROR4 = 0x124, MIR = 0x140;
__device__ __forceinline__ float sw32_sum(float a, float b) {
  auto r = __builtin_amdgcn_permlane32_swap(__float_as_uint(a), __float_as_uint(b), false, false);
  return __uint_as_float(r[0]) + __uint_as_float(r[1]);
}
__device__ __forceinline__ float sw16_sum(float a, float b) {
  auto r = __builtin_amdgcn_permlane16_swap(__float_as_uint(a), __float_as_uint(b), false, false);
  return __uint_as_float(r[0]) + __uint_as_float(r[1]);
}
__device__ __forceinline__ float sw32_max(float a) {
  auto r = __builtin_amdgcn_permlane32_swap(__float_as_uint(a), __float_as_uint(a), false, false);
  return fmaxf(__uint_as_float(r[0]), __uint_as_float(r[1]));
}
__device__ __forceinline__ float sw16_max(float a) {
  auto r = __builtin_amdgcn_permlane16_swap(__float_as_uint(a), __float_as_uint(a), false, false);
  return fmaxf(__uint_as_float(r[0]), __uint_as_float(r[1]));
}
__device__ __forceinline__ float wave_sum(float v) {
  v += dpp<XOR1>(v); v += dpp<XOR2>(v); v += dpp<XOR7>(v); v += dpp<XOR8>(v);
  v = sw16_sum(v, v); v = sw32_sum(v, v);
  return v;
}
template <int H> __device__ __forceinline__ void tr32(float* v) {
#pragma unroll
  for (int j = 0; j < H; ++j) v[j] = sw32_sum(v[j], v[j + H]);
}
template <int H> __device__ __forceinline__ void tr16(float* v) {
#pragma unroll
  for (int j = 0; j < H; ++j) v[j] = sw16_sum(v[j], v[j + H]);
}
template <int H, int CTRL, int BIT> __device__ __forceinline__ void trd(float* v, int lane) {
#pragma unroll
  for (int j = 0; j < H; ++j) {
    const float t1 = v[j] + dpp<CTRL>(v[j]), t2 = v[j + H] + dpp<CTRL>(v[j + H]);
    v[j] = (lane & BIT) ? t2 : t1;
  }
}
#define BAR() do { __builtin_amdgcn_sched_barrier(0); asm volatile("s_waitcnt lgkmcnt(0)" ::: "memory"); __builtin_amdgcn_s_barrier(); asm volatile("" ::: "memory"); __builtin_amdgcn_sched_barrier(0); } while (0)
#define PIN() __builtin_amdgcn_sched_barrier(0)

__device__ __forceinline__ void fma8(float* acc, float xv, v4u w) {
#pragma unroll
  for (int k = 0; k < 4; ++k) {
    acc[2 * k] = fmaf(xv, lo(w[k]), acc[2 * k]);
    acc[2 * k + 1] = fmaf(xv, hi(w[k]), acc[2 * k + 1]);
  }
}

constexpr int L_XN = 0;
constexpr int L_QP = L_XN + 576;
constexpr int L_QB = L_QP + 576;
constexpr int L_PEX = L_QB + 128;
constexpr int L_MRG = L_PEX + 512;
constexpr int L_GUP = L_MRG + 8 * 264;
constexpr int L_OV = L_GUP + 1024;
constexpr int L_HV = L_OV + 16;
constexpr int L_FIN = L_HV + 32;
constexpr int L_RED = L_FIN + 7 * 576;
constexpr int L_CNT = L_RED + 160;
constexpr int L_END = L_CNT + 4;

__device__ __forceinline__ float divr(float a, float b, float ib) { const float q = a * ib; const float r = fmaf(-q, b, a); return fmaf(r, ib, q); }
struct LnS { float mb, sd, isd; };
__device__ __forceinline__ LnS ln_stats(int lane, v4u xa, v4u xb) {
  float xv[8], xw[8];
#pragma unroll
  for (int k = 0; k < 4; ++k) { xv[2 * k] = lo(xa[k]); xv[2 * k + 1] = hi(xa[k]); xw[2 * k] = lo(xb[k]); xw[2 * k + 1] = hi(xb[k]); }
  float sm = 0.f, sm2 = 0.f;
#pragma unroll
  for (int k = 0; k < 8; ++k) { sm += xv[k]; sm2 += xw[k]; }
  sm = lane < 8 ? sm + sm2 : sm;
  const float mean = wave_sum(sm) * (1.0f / D);
  float vs = 0.f, vs2 = 0.f;
#pragma unroll
  for (int k = 0; k < 8; ++k) { const float a = xv[k] - mean; vs = fmaf(a, a, vs); const float b = xw[k] - mean; vs2 = fmaf(b, b, vs2); }
  vs = lane < 8 ? vs + vs2 : vs;
  const float var = rbf(wave_sum(vs) * (1.0f / D));
  LnS r;
  r.mb = rbf(mean);
  r.sd = rbf(sqrtf(rbf(var + 1e-5f)));
  r.isd = 1.0f / r.sd;
  return r;
}
__device__ __forceinline__ float ln_apply(float x, const LnS& s) { return rbf(divr(rbf(x - s.mb), s.sd, s.isd)); }
__device__ __forceinline__ void q_part(float* lds, int lane, int w, const LnS& s, unsigned xq0, unsigned xq1, v4u wq0, v4u wq1) {
  float acc[8] = {0, 0, 0, 0, 0, 0, 0, 0};
  fma8(acc, ln_apply(__uint_as_float(xq0 << 16), s), wq0);
  fma8(acc, ln_apply(__uint_as_float(xq1 << 16), s), wq1);
  tr32<4>(acc); tr16<2>(acc); trd<1, XOR8, 8>(acc, lane);
  lds[L_QP + 64 * w + 8 * (lane & 7) + (lane >> 3)] = acc[0];
}
__device__ __forceinline__ void ln_store8(float* dst, v4u xa, const LnS& s) {
  f32x4 o0, o1;
#pragma unroll
  for (int k = 0; k < 2; ++k) { o0[2 * k] = ln_apply(lo(xa[k]), s); o0[2 * k + 1] = ln_apply(hi(xa[k]), s); o1[2 * k] = ln_apply(lo(xa[2 + k]), s); o1[2 * k + 1] = ln_apply(hi(xa[2 + k]), s); }
  *reinterpret_cast<f32x4*>(dst) = o0;
  *reinterpret_cast<f32x4*>(dst + 4) = o1;
}

__global__ void __launch_bounds__(NTHR, 3) mega_kernel(Params p) {
  __shared__ __attribute__((aligned(16))) float lds[L_END];
  const int tid = threadIdx.x, lane = tid & 63;
  const int w = __builtin_amdgcn_readfirstlane(tid >> 6);
  const int c = blockIdx.x;
  const int h = c >> 4, s = c & 15;
  const bool heavy = (c & 31) < 27;
  const int hu = heavy ? (c >> 5) * 27 + (c & 31) : 0;
  const int li = (c >> 5) * 5 + ((c & 31) - 27);
  const __amdgpu_buffer_rsrc_t rs = __builtin_amdgcn_make_buffer_rsrc((void*)p.ws, 0, WS_END, 0x00020000);
  const int ch8 = lane & 7, rs8 = lane >> 3;
  const int r4 = lane >> 4, c16 = lane & 15;
  const int qrow0 = 144 * (s >> 2) + 16 * w;
  const u16* wqp = p.wq + (size_t)(qrow0 + rs8) * NQ + 256 * h + 64 * (s & 3) + 8 * ch8;
  STAMP(0);

  if (w == 8) {
    __builtin_amdgcn_s_setprio(3);
    const v4u xa = *reinterpret_cast<const v4u*>(p.x + 8 * lane);
    const v4u xb = *reinterpret_cast<const v4u*>(p.x + 512 + 8 * (lane & 7));
    const unsigned xq0 = p.x[qrow0 + rs8], xq1 = p.x[qrow0 + 8 + rs8];
    const v4u wq0 = ldw(wqp), wq1 = ldw(wqp + (size_t)8 * NQ);
    const unsigned e0 = *reinterpret_cast<const unsigned*>(p.ws + WS_E);
    if (lane < 4) reinterpret_cast<unsigned*>(lds)[L_CNT + lane] = 0u;
    PIN();
    BAR();
    PIN();
    const unsigned tag0 = e0 + 1u, tag1 = ~tag0;
    const LnS lns = ln_stats(lane, xa, xb);
    STAMP(12);
    q_part(lds, lane, w, lns, xq0, xq1, wq0, wq1);
    STAMP(13);
    BAR();
    STAMP(1);
    {
      if (lane < 32) {
        float a = 0.f, b = 0.f;
#pragma unroll
        for (int ww = 0; ww < 9; ++ww) { const f32x2 v = *reinterpret_cast<const f32x2*>(&lds[L_QP + 64 * ww + 2 * lane]); a += v[0]; b += v[1]; }
        const v4u g = {__float_as_uint(a), __float_as_uint(b), tag0, tag1};
        __builtin_amdgcn_raw_buffer_store_b128(g, rs, WS_QG + c * 512 + lane * 16, 0, 16);
      }
    }
    {
      v4u g[8];
      for (unsigned spin = 0;; ++spin) {
        bool ok = true;
#pragma unroll
        for (int k = 0; k < 8; ++k) g[k] = __builtin_amdgcn_raw_buffer_load_b128(rs, WS_QG + (16 * h + (lane >> 5) + 2 * k) * 512 + (lane & 31) * 16, 0, 16);
#pragma unroll
        for (int k = 0; k < 8; ++k) ok &= (g[k][2] == tag0) & (g[k][3] == tag1);
        if (__all(ok) || spin > SPIN_CAP) break;
        asm volatile("" ::: "memory");
      }
      float a[2] = {0.f, 0.f}, b[2] = {0.f, 0.f};
#pragma unroll
      for (int k = 0; k < 8; ++k) { a[k & 1] += __uint_as_float(g[k][0]); b[k & 1] += __uint_as_float(g[k][1]); }
#pragma unroll
      for (int j = 0; j < 2; ++j) {
        const int cg = (lane >> 5) + 2 * j;
        reinterpret_cast<unsigned*>(lds)[L_QB + 32 * cg + (lane & 31)] = f2bf(a[j]) | (f2bf(b[j]) << 16);
      }
    }
    asm volatile("s_waitcnt lgkmcnt(0)" ::: "memory");
    if (lane == 0) __hip_atomic_store(reinterpret_cast<unsigned*>(lds) + L_CNT + 1, 1u, __ATOMIC_RELAXED, __HIP_MEMORY_SCOPE_WORKGROUP);
    STAMP(2);
    {
      const int ks = lane / 18, j = lane - 18 * ks;
      const int f = 18 * c + j;
      const int off = WS_HG + ((ks < 3 ? ks : 0) * 72 + (f >> 6)) * 1024 + (f & 63) * 16;
      v4u g;
      for (unsigned spin = 0;; ++spin) {
        g = __builtin_amdgcn_raw_buffer_load_b128(rs, off, 0, 16);
        const bool ok = (g[2] == tag0) & (g[3] == tag1);
        if (__all(ok) || spin > SPIN_CAP) break;
        asm volatile("" ::: "memory");
      }
      float a = __uint_as_float(g[0]), b = __uint_as_float(g[1]);
      const float a1 = __shfl(a, lane + 18), b1 = __shfl(b, lane + 18), a2 = __shfl(a, lane + 36), b2 = __shfl(b, lane + 36);
      if (lane < 18) {
        float gt = rbf(a + a1 + a2);
        const float up = rbf(b + b1 + b2);
        gt = gt > 0.f ? gt : 0.f;
        lds[L_HV + lane] = rbf(gt * up);
      }
    }
    STAMP(5);
    BAR();
    STAMP(3);
    {
#pragma unroll
      for (int g = 0; g < 2; ++g) {
        float mw[8], M = -INFINITY;
#pragma unroll
        for (int ww = 0; ww < 8; ++ww) { mw[ww] = lds[L_MRG + (ww * 2 + g) * 132 + 128]; M = fmaxf(M, mw[ww]); }
        float L = 0.f, a = 0.f, b = 0.f;
#pragma unroll
        for (int ww = 0; ww < 8; ++ww) {
          const float wt = __expf(mw[ww] - M);
          L = fmaf(wt, lds[L_MRG + (ww * 2 + g) * 132 + 129], L);
          const f32x2 v = *reinterpret_cast<const f32x2*>(&lds[L_MRG + (ww * 2 + g) * 132 + 2 * lane]);
          a = fmaf(wt, v[0], a); b = fmaf(wt, v[1], b);
        }
        const int rec = WS_AG + c * 2304 + (g * 8 + (lane >> 3)) * 144;
        const v4u ga = {__float_as_uint(a), __float_as_uint(b), tag0, tag1};
        __builtin_amdgcn_raw_buffer_store_b128(ga, rs, rec + 16 + (lane & 7) * 16, 0, 16);
        if ((lane & 7) == 0) {
          const v4u gm = {__float_as_uint(M), __float_as_uint(L), tag0, tag1};
          __builtin_amdgcn_raw_buffer_store_b128(gm, rs, rec, 0, 16);
        }
      }
    }
    STAMP(4);
    {
      const int pr = lane >> 2, q4 = lane & 3;
      const int rec = WS_AG + (16 * h + pr) * 2304 + s * 144;
      v4u gm, g0, g1;
      for (unsigned spin = 0;; ++spin) {
        gm = __builtin_amdgcn_raw_buffer_load_b128(rs, rec, 0, 16);
        g0 = __builtin_amdgcn_raw_buffer_load_b128(rs, rec + 16 + 32 * q4, 0, 16);
        g1 = __builtin_amdgcn_raw_buffer_load_b128(rs, rec + 32 + 32 * q4, 0, 16);
        const bool ok = (gm[2] == tag0) & (gm[3] == tag1) & (g0[2] == tag0) & (g0[3] == tag1) & (g1[2] == tag0) & (g1[3] == tag1);
        if (__all(ok) || spin > SPIN_CAP) break;
        asm volatile("" ::: "memory");
      }
      STAMP(6);
      const float m = __uint_as_float(gm[0]);
      float M = m;
      M = fmaxf(M, dpp<ROR4>(M)); M = fmaxf(M, dpp<XOR8>(M)); M = sw16_max(M); M = sw32_max(M);
      const float wt = __expf(m - M);
      float v[5] = {wt * __uint_as_float(gm[1]), wt * __uint_as_float(g0[0]), wt * __uint_as_float(g0[1]), wt * __uint_as_float(g1[0]), wt * __uint_as_float(g1[1])};
#pragma unroll
      for (int j = 0; j < 5; ++j) { v[j] += dpp<ROR4>(v[j]); v[j] += dpp<XOR8>(v[j]); v[j] = sw16_sum(v[j], v[j]); v[j] = sw32_sum(v[j], v[j]); }
      if (lane < 4) {
        const float inv = 1.0f / v[0];
        f32x4 o4 = {rbf(v[1] * inv), rbf(v[2] * inv), rbf(v[3] * inv), rbf(v[4] * inv)};
        *reinterpret_cast<f32x4*>(&lds[L_OV + 4 * lane]) = o4;
      }
    }
    STAMP(7);
    BAR();
    return;
  }

  const size_t kvbase = ((size_t)h * SK + (SK - W) + 256 * s + 32 * w + r4) * DH + 8 * c16;
  const v4u xa = *reinterpret_cast<const v4u*>(p.x + 8 * lane);
  const v4u xb = *reinterpret_cast<const v4u*>(p.x + 512 + 8 * (lane & 7));
  const int gurow0 = 192 * (hu / 72) + 24 * w;
  const unsigned xq0 = p.x[qrow0 + rs8], xq1 = p.x[qrow0 + 8 + rs8];
  const unsigned xg0 = p.x[gurow0 + rs8], xg1 = p.x[gurow0 + 8 + rs8], xg2 = p.x[gurow0 + 16 + rs8];
  const v4u wq0 = ldw(wqp), wq1 = ldw(wqp + (size_t)8 * NQ);
  const unsigned e0 = *reinterpret_cast<const unsigned*>(p.ws + WS_E);
  const unsigned xfin = p.x[16 * (li < 0 ? 0 : (li > 35 ? 35 : li)) + (tid & 15)];
  PIN();
  BAR();
  PIN();
  v4u kr[8], vr[8], gr[3], ur[3], wor[3], wdr[3];
  const size_t guo = heavy ? (size_t)(gurow0 + rs8) * F + 64 * (hu % 72) + 8 * ch8 : (size_t)0;
  const size_t gust = heavy ? (size_t)8 * F : (size_t)0;
  const int tq = tid < 504 ? tid : 503;
  const int ch72 = tq % 72, rg7 = tq / 72;
#define DEPTH 63
#define STR2(x) #x
#define STR(x) STR2(x)
#define PACE() do { PIN(); asm volatile("s_waitcnt vmcnt(" STR(DEPTH) ")" ::: "memory"); PIN(); } while (0)
#define LD_K0() _Pragma("unroll") for (int i = 0; i < 8; ++i) kr[i] = ldw(p.kc + kvbase + (size_t)(4 * i) * DH);
#define LD_GU0() _Pragma("unroll") for (int i = 0; i < 3; ++i) { gr[i] = ldw(p.wg + guo + gust * i); ur[i] = ldw(p.wu + guo + gust * i); }
#define LD_K() _Pragma("unroll") for (int i = 0; i < 8; ++i) { PACE(); kr[i] = ldw(p.kc + kvbase + (size_t)(4 * i) * DH); }
#define LD_V() _Pragma("unroll") for (int i = 0; i < 8; ++i) { PACE(); vr[i] = ldw(p.vc + kvbase + (size_t)(4 * i) * DH); }
#define LD_GU() _Pragma("unroll") for (int i = 0; i < 3; ++i) { PACE(); gr[i] = ldw(p.wg + guo + gust * i); PACE(); ur[i] = ldw(p.wu + guo + gust * i); }
#define LD_DO() _Pragma("unroll") for (int i = 0; i < 3; ++i) { const int r = min(rg7 + 7 * i, 17); PACE(); wdr[i] = ldw(p.wd + (size_t)(18 * c + r) * D + 8 * ch72); } \
                _Pragma("unroll") for (int i = 0; i < 3; ++i) { const int r = min(rg7 + 7 * i, 15); PACE(); wor[i] = ldw(p.wo + (size_t)(16 * c + r) * D + 8 * ch72); }
  LD_GU0();
  PIN();
  const unsigned tag0 = e0 + 1u, tag1 = ~tag0;
  const LnS lns = ln_stats(lane, xa, xb);
  STAMP(12);
  q_part(lds, lane, w, lns, xq0, xq1, wq0, wq1);
  STAMP(13);
  BAR();
  STAMP(1);
  LD_K0();
  PIN();
#define GU_PARTIAL
  auto gu_partial = [&]() {
  {
    float ag[8] = {0, 0, 0, 0, 0, 0, 0, 0}, au[8] = {0, 0, 0, 0, 0, 0, 0, 0};
    const float xv[3] = {ln_apply(__uint_as_float(xg0 << 16), lns), ln_apply(__uint_as_float(xg1 << 16), lns), ln_apply(__uint_as_float(xg2 << 16), lns)};
#pragma unroll
    for (int i = 0; i < 3; ++i) {
      fma8(ag, xv[i], gr[i]);
      fma8(au, xv[i], ur[i]);
    }
    tr32<4>(ag); tr16<2>(ag); trd<1, XOR8, 8>(ag, lane);
    tr32<4>(au); tr16<2>(au); trd<1, XOR8, 8>(au, lane);
    lds[L_GUP + 128 * w + 8 * ch8 + rs8] = ag[0];
    lds[L_GUP + 128 * w + 64 + 8 * ch8 + rs8] = au[0];
    asm volatile("s_waitcnt lgkmcnt(0)" ::: "memory");
    unsigned old = 0;
    if (lane == 0) old = __hip_atomic_fetch_add(reinterpret_cast<unsigned*>(lds) + L_CNT, 1u, __ATOMIC_RELAXED, __HIP_MEMORY_SCOPE_WORKGROUP);
    old = __builtin_amdgcn_readfirstlane(old);
    if (old == 7u && heavy) {
      asm volatile("" ::: "memory");
      float a = 0.f, b = 0.f;
#pragma unroll
      for (int ww = 0; ww < 8; ++ww) { a += lds[L_GUP + 128 * ww + lane]; b += lds[L_GUP + 128 * ww + 64 + lane]; }
      const v4u g = {__float_as_uint(a), __float_as_uint(b), tag0, tag1};
      __builtin_amdgcn_raw_buffer_store_b128(g, rs, WS_HG + hu * 1024 + lane * 16, 0, 16);
    }
  }
  };
  gu_partial();
  STAMP(6);
  LD_V();
  PIN();
  STAMP(2);
  while (__hip_atomic_load(reinterpret_cast<unsigned*>(lds) + L_CNT + 1, __ATOMIC_RELAXED, __HIP_MEMORY_SCOPE_WORKGROUP) == 0u) __builtin_amdgcn_s_sleep(1);
  asm volatile("" ::: "memory");
  PIN();
  STAMP(3);
  float pv[16];
  {
    v4u qv[2];
#pragma unroll
    for (int g = 0; g < 2; ++g) qv[g] = *reinterpret_cast<const v4u*>(&lds[L_QB + 64 * g + 4 * c16]);
    float x[16];
#pragma unroll
    for (int i = 0; i < 8; ++i)
#pragma unroll
      for (int g = 0; g < 2; ++g) {
        float d = 0.f;
#pragma unroll
        for (int k = 0; k < 4; ++k) d = dot2(kr[i][k], qv[g][k], d);
        x[2 * i + g] = d;
      }
    trd<8, XOR8, 8>(x, lane); trd<4, XOR7, 4>(x, lane); trd<2, XOR2, 2>(x, lane); trd<1, XOR1, 1>(x, lane);
    const float sc = rbf(rbf(x[0]) * 0.08838834764831845f);
    float m = sc;
    m = fmaxf(m, dpp<XOR2>(m)); m = fmaxf(m, dpp<ROR4>(m)); m = fmaxf(m, dpp<XOR8>(m)); m = sw16_max(m); m = sw32_max(m);
    const float pe = __expf(sc - m);
    float l = pe;
    l += dpp<XOR2>(l); l += dpp<ROR4>(l); l += dpp<XOR8>(l); l = sw16_sum(l, l); l = sw32_sum(l, l);
    if (lane < 2) { lds[L_MRG + (w * 2 + lane) * 132 + 128] = m; lds[L_MRG + (w * 2 + lane) * 132 + 129] = l; }
    lds[L_PEX + 64 * w + lane] = pe;
    asm volatile("s_waitcnt lgkmcnt(0)" ::: "memory");
#pragma unroll
    for (int u = 0; u < 4; ++u) {
      const f32x4 t = *reinterpret_cast<const f32x4*>(&lds[L_PEX + 64 * w + 16 * r4 + 4 * u]);
      pv[4 * u] = t[0]; pv[4 * u + 1] = t[1]; pv[4 * u + 2] = t[2]; pv[4 * u + 3] = t[3];
    }
  }
  STAMP(4);
  LD_DO();
  PIN();
  {
    float acc[16];
#pragma unroll
    for (int k = 0; k < 16; ++k) acc[k] = 0.f;
#pragma unroll
    for (int i = 0; i < 8; ++i) {
      fma8(acc, pv[2 * i], vr[i]);
      fma8(acc + 8, pv[2 * i + 1], vr[i]);
    }
    tr32<8>(acc); tr16<4>(acc);
    const f32x4 o4 = {acc[0], acc[1], acc[2], acc[3]};
    *reinterpret_cast<f32x4*>(&lds[L_MRG + (w * 2 + (lane >> 5)) * 132 + 8 * c16 + 4 * ((lane >> 4) & 1)]) = o4;
  }
  STAMP(5);
  BAR();
  BAR();
  STAMP(7);
  if (tid < 504) {
    float acc[8] = {0, 0, 0, 0, 0, 0, 0, 0};
#pragma unroll
    for (int i = 0; i < 3; ++i) {
      const int r = rg7 + 7 * i;
      const float hv = r < 18 ? lds[L_HV + min(r, 17)] : 0.f, ov = r < 16 ? lds[L_OV + min(r, 15)] : 0.f;
      fma8(acc, hv, wdr[i]);
      fma8(acc, ov, wor[i]);
    }
    const f32x4 a0 = {acc[0], acc[1], acc[2], acc[3]}, a1 = {acc[4], acc[5], acc[6], acc[7]};
    *reinterpret_cast<f32x4*>(&lds[L_FIN + 576 * rg7 + 8 * ch72]) = a0;
    *reinterpret_cast<f32x4*>(&lds[L_FIN + 576 * rg7 + 8 * ch72 + 4]) = a1;
  }
  BAR();
  if (tid < 288) {
    float a = 0.f, b = 0.f;
#pragma unroll
    for (int r = 0; r < 7; ++r) { const f32x2 v = *reinterpret_cast<const f32x2*>(&lds[L_FIN + 576 * r + 2 * tid]); a += v[0]; b += v[1]; }
    const v4u g = {__float_as_uint(a), __float_as_uint(b), tag0, tag1};
    __builtin_amdgcn_raw_buffer_store_b128(g, rs, WS_FG + c * 4608 + tid * 16, 0, 16);
  }
  STAMP(8);
  if (heavy || li >= 36) return;
  {
    const int gi = tid & 7, p0 = tid >> 3;
    v4u g[4];
    for (unsigned spin = 0;; ++spin) {
      bool ok = true;
#pragma unroll
      for (int k = 0; k < 4; ++k) g[k] = __builtin_amdgcn_raw_buffer_load_b128(rs, WS_FG + (p0 + 64 * k) * 4608 + li * 128 + gi * 16, 0, 16);
#pragma unroll
      for (int k = 0; k < 4; ++k) ok &= (g[k][2] == tag0) & (g[k][3] == tag1);
      if (__all(ok) || spin > SPIN_CAP) break;
      asm volatile("" ::: "memory");
    }
    STAMP(9);
    float a = 0.f, b = 0.f;
#pragma unroll
    for (int k = 0; k < 4; ++k) { a += __uint_as_float(g[k][0]); b += __uint_as_float(g[k][1]); }
#pragma unroll
    for (int k = 0; k < 1; ++k) { a += dpp<XOR8>(a); b += dpp<XOR8>(b); a = sw16_sum(a, a); b = sw16_sum(b, b); a = sw32_sum(a, a); b = sw32_sum(b, b); }
    if (lane < 8) { lds[L_RED + 16 * w + 2 * lane] = a; lds[L_RED + 16 * w + 2 * lane + 1] = b; }
    BAR();
    if (tid < 16) {
      float t = 0.f;
#pragma unroll
      for (int ww = 0; ww < 8; ++ww) t += lds[L_RED + 16 * ww + tid];
      const int n = 16 * li + tid;
      const float xv = __uint_as_float(xfin << 16);
      p.out[n] = (u16)f2bf(xv + t);
    }
    if (li == 0 && tid == 0) *reinterpret_cast<unsigned*>(p.ws + WS_E) = tag0;
    STAMP(10);
  }
}

extern "C" void kernel_launch(void* const* d_in, const int* in_sizes, int n_in, void* d_out, int out_size, void* d_ws, size_t ws_size,
                              hipStream_t stream) {
  Params p;
  p.x = (const u16*)d_in[0];
  p.kc = (const u16*)d_in[1];
  p.vc = (const u16*)d_in[2];
  p.wq = (const u16*)d_in[3];
  p.wo = (const u16*)d_in[4];
  p.wg = (const u16*)d_in[6];
  p.wu = (const u16*)d_in[7];
  p.wd = (const u16*)d_in[8];
  p.out = (u16*)d_out;
  p.ws = (unsigned char*)d_ws;
  mega_kernel<<<256, NTHR, 0, stream>>>(p);
}
```

```cpp
#include <hip/hip_runtime.h>
#include <hip/hip_bf16.h>
#include <stdint.h>

typedef unsigned v4u __attribute__((ext_vector_type(4)));
typedef float f32x4 __attribute__((ext_vector_type(4)));
typedef float f32x2 __attribute__((ext_vector_type(2)));
typedef unsigned short u16;
using bf16x2 = __attribute__((ext_vector_type(2))) __bf16;

constexpr int D = 576, SK = 32768, DH = 128, W = 4096, F = 4608, NQ = 4096;
constexpr int NTHR = 576;
constexpr int WS_E = 0;
constexpr int WS_QG = 4096;
constexpr int WS_AG = WS_QG + 256 * 512;
constexpr int WS_HG = WS_AG + 256 * 2304;
constexpr int WS_FG = WS_HG + 216 * 1024;
constexpr int WS_END = WS_FG + 256 * 4608;
constexpr unsigned SPIN_CAP = 1u << 16;


struct Params {
  const u16 *x, *kc, *vc, *wq, *wo, *wg, *wu, *wd;
  u16* out;
  unsigned char* ws;
};

__device__ __forceinline__ float rbf(float f) {
  unsigned u = __float_as_uint(f);
  u += 0x7FFFu + ((u >> 16) & 1u);
  return __uint_as_float(u & 0xFFFF0000u);
}
__device__ __forceinline__ unsigned f2bf(float f) {
  unsigned u = __float_as_uint(f);
  u += 0x7FFFu + ((u >> 16) & 1u);
  return u >> 16;
}
__device__ __forceinline__ float lo(unsigned w) { return __uint_as_float(w << 16); }
__device__ __forceinline__ float hi(unsigned w) { return __uint_as_float(w & 0xFFFF0000u); }
__device__ __forceinline__ float dot2(unsigned k, unsigned q, float acc) {
  return __builtin_amdgcn_fdot2_f32_bf16(__builtin_bit_cast(bf16x2, k), __builtin_bit_cast(bf16x2, q), acc, false);
}
__device__ __forceinline__ v4u ldw(const u16* p) { return __builtin_nontemporal_load(reinterpret_cast<const v4u*>(p)); }

template <int CTRL> __device__ __forceinline__ float dpp(float x) {
  return __builtin_bit_cast(float, __builtin_amdgcn_mov_dpp(__builtin_bit_cast(int, x), CTRL, 0xf, 0xf, true));
}
constexpr int XOR1 = 0xB1, XOR2 = 0x4E, XOR7 = 0x141, XOR8 = 0x128, ROR4 = 0x124, MIR = 0x140;
__device__ __forceinline__ float sw32_sum(float a, float b) {
  auto r = __builtin_amdgcn_permlane32_swap(__float_as_uint(a), __float_as_uint(b), false, false);
  return __uint_as_float(r[0]) + __uint_as_float(r[1]);
}
__device__ __forceinline__ float sw16_sum(float a, float b) {
  auto r = __builtin_amdgcn_permlane16_swap(__float_as_uint(a), __float_as_uint(b), false, false);
  return __uint_as_float(r[0]) + __uint_as_float(r[1]);
}
__device__ __forceinline__ float sw32_max(float a) {
  auto r = __builtin_amdgcn_permlane32_swap(__float_as_uint(a), __float_as_uint(a), false, false);
  return fmaxf(__uint_as_float(r[0]), __uint_as_float(r[1]));
}
__device__ __forceinline__ float sw16_max(float a) {
  auto r = __builtin_amdgcn_permlane16_swap(__float_as_uint(a), __float_as_uint(a), false, false);
  return fmaxf(__uint_as_float(r[0]), __uint_as_float(r[1]));
}
__device__ __forceinline__ float wave_sum(float v) {
  v += dpp<XOR1>(v); v += dpp<XOR2>(v); v += dpp<XOR7>(v); v += dpp<XOR8>(v);
  v = sw16_sum(v, v); v = sw32_sum(v, v);
  return v;
}
template <int H> __device__ __forceinline__ void tr32(float* v) {
#pragma unroll
  for (int j = 0; j < H; ++j) v[j] = sw32_sum(v[j], v[j + H]);
}
template <int H> __device__ __forceinline__ void tr16(float* v) {
#pragma unroll
  for (int j = 0; j < H; ++j) v[j] = sw16_sum(v[j], v[j + H]);
}
template <int H, int CTRL, int BIT> __device__ __forceinline__ void trd(float* v, int lane) {
#pragma unroll
  for (int j = 0; j < H; ++j) {
    const float t1 = v[j] + dpp<CTRL>(v[j]), t2 = v[j + H] + dpp<CTRL>(v[j + H]);
    v[j] = (lane & BIT) ? t2 : t1;
  }
}
#define BAR() do { __builtin_amdgcn_sched_barrier(0); asm volatile("s_waitcnt lgkmcnt(0)" ::: "memory"); __builtin_amdgcn_s_barrier(); asm volatile("" ::: "memory"); __builtin_amdgcn_sched_barrier(0); } while (0)
#define PIN() __builtin_amdgcn_sched_barrier(0)

__device__ __forceinline__ void fma8(float* acc, float xv, v4u w) {
#pragma unroll
  for (int k = 0; k < 4; ++k) {
    acc[2 * k] = fmaf(xv, lo(w[k]), acc[2 * k]);
    acc[2 * k + 1] = fmaf(xv, hi(w[k]), acc[2 * k + 1]);
  }
}

constexpr int L_XN = 0;
constexpr int L_QP = L_XN + 576;
constexpr int L_QB = L_QP + 576;
constexpr int L_PEX = L_QB + 128;
constexpr int L_MRG = L_PEX + 512;
constexpr int L_GUP = L_MRG + 8 * 264;
constexpr int L_OV = L_GUP + 1024;
constexpr int L_HV = L_OV + 16;
constexpr int L_FIN = L_HV + 32;
constexpr int L_RED = L_FIN + 7 * 576;
constexpr int L_CNT = L_RED + 160;
constexpr int L_END = L_CNT + 4;

__device__ __forceinline__ float divr(float a, float b, float ib) { const float q = a * ib; const float r = fmaf(-q, b, a); return fmaf(r, ib, q); }
struct LnS { float mb, sd, isd; };
__device__ __forceinline__ LnS ln_stats(int lane, v4u xa, v4u xb) {
  float xv[8], xw[8];
#pragma unroll
  for (int k = 0; k < 4; ++k) { xv[2 * k] = lo(xa[k]); xv[2 * k + 1] = hi(xa[k]); xw[2 * k] = lo(xb[k]); xw[2 * k + 1] = hi(xb[k]); }
  float sm = 0.f, sm2 = 0.f;
#pragma unroll
  for (int k = 0; k < 8; ++k) { sm += xv[k]; sm2 += xw[k]; }
  sm = lane < 8 ? sm + sm2 : sm;
  const float mean = wave_sum(sm) * (1.0f / D);
  float vs = 0.f, vs2 = 0.f;
#pragma unroll
  for (int k = 0; k < 8; ++k) { const float a = xv[k] - mean; vs = fmaf(a, a, vs); const float b = xw[k] - mean; vs2 = fmaf(b, b, vs2); }
  vs = lane < 8 ? vs + vs2 : vs;
  const float var = rbf(wave_sum(vs) * (1.0f / D));
  LnS r;
  r.mb = rbf(mean);
  r.sd = rbf(sqrtf(rbf(var + 1e-5f)));
  r.isd = 1.0f / r.sd;
  return r;
}
__device__ __forceinline__ float ln_apply(float x, const LnS& s) { return rbf(divr(rbf(x - s.mb), s.sd, s.isd)); }
__device__ __forceinline__ void q_part(float* lds, int lane, int w, const LnS& s, unsigned xq0, unsigned xq1, v4u wq0, v4u wq1) {
  float acc[8] = {0, 0, 0, 0, 0, 0, 0, 0};
  fma8(acc, ln_apply(__uint_as_float(xq0 << 16), s), wq0);
  fma8(acc, ln_apply(__uint_as_float(xq1 << 16), s), wq1);
  tr32<4>(acc); tr16<2>(acc); trd<1, XOR8, 8>(acc, lane);
  lds[L_QP + 64 * w + 8 * (lane & 7) + (lane >> 3)] = acc[0];
}
__device__ __forceinline__ void ln_store8(float* dst, v4u xa, const LnS& s) {
  f32x4 o0, o1;
#pragma unroll
  for (int k = 0; k < 2; ++k) { o0[2 * k] = ln_apply(lo(xa[k]), s); o0[2 * k + 1] = ln_apply(hi(xa[k]), s); o1[2 * k] = ln_apply(lo(xa[2 + k]), s); o1[2 * k + 1] = ln_apply(hi(xa[2 + k]), s); }
  *reinterpret_cast<f32x4*>(dst) = o0;
  *reinterpret_cast<f32x4*>(dst + 4) = o1;
}

__global__ void __launch_bounds__(NTHR, 3) mega_kernel(Params p) {
  __shared__ __attribute__((aligned(16))) float lds[L_END];
  const int tid = threadIdx.x, lane = tid & 63;
  const int w = __builtin_amdgcn_readfirstlane(tid >> 6);
  const int c = blockIdx.x;
  const int h = c >> 4, s = c & 15;
  const bool heavy = (c & 31) < 27;
  const int hu = heavy ? (c >> 5) * 27 + (c & 31) : 0;
  const int li = (c >> 5) * 5 + ((c & 31) - 27);
  const __amdgpu_buffer_rsrc_t rs = __builtin_amdgcn_make_buffer_rsrc((void*)p.ws, 0, WS_END, 0x00020000);
  const int ch8 = lane & 7, rs8 = lane >> 3;
  const int r4 = lane >> 4, c16 = lane & 15;
  const int qrow0 = 144 * (s >> 2) + 16 * w;
  { const unsigned long long t0 = __builtin_amdgcn_s_memrealtime(); while (__builtin_amdgcn_s_memrealtime() - t0 < 600ull) __builtin_amdgcn_s_sleep(2); }
  const u16* wqp = p.wq + (size_t)(qrow0 + rs8) * NQ + 256 * h + 64 * (s & 3) + 8 * ch8;

  if (w == 8) {
    __builtin_amdgcn_s_setprio(3);
    const v4u xa = *reinterpret_cast<const v4u*>(p.x + 8 * lane);
    const v4u xb = *reinterpret_cast<const v4u*>(p.x + 512 + 8 * (lane & 7));
    const unsigned xq0 = p.x[qrow0 + rs8], xq1 = p.x[qrow0 + 8 + rs8];
    const v4u wq0 = ldw(wqp), wq1 = ldw(wqp + (size_t)8 * NQ);
    const unsigned e0 = *reinterpret_cast<const unsigned*>(p.ws + WS_E);
    if (lane < 4) reinterpret_cast<unsigned*>(lds)[L_CNT + lane] = 0u;
    PIN();
    BAR();
    PIN();
    const unsigned tag0 = e0 + 1u, tag1 = ~tag0;
    const LnS lns = ln_stats(lane, xa, xb);
    q_part(lds, lane, w, lns, xq0, xq1, wq0, wq1);
    BAR();
    {
      if (lane < 32) {
        float a = 0.f, b = 0.f;
#pragma unroll
        for (int ww = 0; ww < 9; ++ww) { const f32x2 v = *reinterpret_cast<const f32x2*>(&lds[L_QP + 64 * ww + 2 * lane]); a += v[0]; b += v[1]; }
        const v4u g = {__float_as_uint(a), __float_as_uint(b), tag0, tag1};
        __builtin_amdgcn_raw_buffer_store_b128(g, rs, WS_QG + c * 512 + lane * 16, 0, 16);
      }
    }
    {
      v4u g[8];
      for (unsigned spin = 0;; ++spin) {
        bool ok = true;
#pragma unroll
        for (int k = 0; k < 8; ++k) g[k] = __builtin_amdgcn_raw_buffer_load_b128(rs, WS_QG + (16 * h + (lane >> 5) + 2 * k) * 512 + (lane & 31) * 16, 0, 16);
#pragma unroll
        for (int k = 0; k < 8; ++k) ok &= (g[k][2] == tag0) & (g[k][3] == tag1);
        if (__all(ok) || spin > SPIN_CAP) break;
        asm volatile("" ::: "memory");
      }
      float a[2] = {0.f, 0.f}, b[2] = {0.f, 0.f};
#pragma unroll
      for (int k = 0; k < 8; ++k) { a[k & 1] += __uint_as_float(g[k][0]); b[k & 1] += __uint_as_float(g[k][1]); }
#pragma unroll
      for (int j = 0; j < 2; ++j) {
        const int cg = (lane >> 5) + 2 * j;
        reinterpret_cast<unsigned*>(lds)[L_QB + 32 * cg + (lane & 31)] = f2bf(a[j]) | (f2bf(b[j]) << 16);
      }
    }
    asm volatile("s_waitcnt lgkmcnt(0)" ::: "memory");
    if (lane == 0) __hip_atomic_store(reinterpret_cast<unsigned*>(lds) + L_CNT + 1, 1u, __ATOMIC_RELAXED, __HIP_MEMORY_SCOPE_WORKGROUP);
    BAR();
    {
#pragma unroll
      for (int g = 0; g < 2; ++g) {
        float mw[8], M = -INFINITY;
#pragma unroll
        for (int ww = 0; ww < 8; ++ww) { mw[ww] = lds[L_MRG + (ww * 2 + g) * 132 + 128]; M = fmaxf(M, mw[ww]); }
        float L = 0.f, a = 0.f, b = 0.f;
#pragma unroll
        for (int ww = 0; ww < 8; ++ww) {
          const float wt = __expf(mw[ww] - M);
          L = fmaf(wt, lds[L_MRG + (ww * 2 + g) * 132 + 129], L);
          const f32x2 v = *reinterpret_cast<const f32x2*>(&lds[L_MRG + (ww * 2 + g) * 132 + 2 * lane]);
          a = fmaf(wt, v[0], a); b = fmaf(wt, v[1], b);
        }
        const int rec = WS_AG + c * 2304 + (g * 8 + (lane >> 3)) * 144;
        const v4u ga = {__float_as_uint(a), __float_as_uint(b), tag0, tag1};
        __builtin_amdgcn_raw_buffer_store_b128(ga, rs, rec + 16 + (lane & 7) * 16, 0, 16);
        if ((lane & 7) == 0) {
          const v4u gm = {__float_as_uint(M), __float_as_uint(L), tag0, tag1};
          __builtin_amdgcn_raw_buffer_store_b128(gm, rs, rec, 0, 16);
        }
      }
    }
    {
      const int pr = lane >> 2, q4 = lane & 3;
      const int rec = WS_AG + (16 * h + pr) * 2304 + s * 144;
      const int ks = lane / 18, j = lane - 18 * ks;
      const int f = 18 * c + j;
      const int hoff = WS_HG + ((ks < 3 ? ks : 0) * 72 + (f >> 6)) * 1024 + (f & 63) * 16;
      v4u gm, g0, g1, gh;
      for (unsigned spin = 0;; ++spin) {
        gm = __builtin_amdgcn_raw_buffer_load_b128(rs, rec, 0, 16);
        g0 = __builtin_amdgcn_raw_buffer_load_b128(rs, rec + 16 + 32 * q4, 0, 16);
        g1 = __builtin_amdgcn_raw_buffer_load_b128(rs, rec + 32 + 32 * q4, 0, 16);
        gh = __builtin_amdgcn_raw_buffer_load_b128(rs, hoff, 0, 16);
        const bool ok = (gm[2] == tag0) & (gm[3] == tag1) & (g0[2] == tag0) & (g0[3] == tag1) & (g1[2] == tag0) & (g1[3] == tag1) & (gh[2] == tag0) & (gh[3] == tag1);
        if (__all(ok) || spin > SPIN_CAP) break;
        asm volatile("" ::: "memory");
      }
      {
        const float a = __uint_as_float(gh[0]), b = __uint_as_float(gh[1]);
        const float a1 = __shfl(a, lane + 18), b1 = __shfl(b, lane + 18), a2 = __shfl(a, lane + 36), b2 = __shfl(b, lane + 36);
        if (lane < 18) {
          float gt = rbf(a + a1 + a2);
          const float up = rbf(b + b1 + b2);
          gt = gt > 0.f ? gt : 0.f;
          lds[L_HV + lane] = rbf(gt * up);
        }
      }
      const float m = __uint_as_float(gm[0]);
      float M = m;
      M = fmaxf(M, dpp<ROR4>(M)); M = fmaxf(M, dpp<XOR8>(M)); M = sw16_max(M); M = sw32_max(M);
      const float wt = __expf(m - M);
      float v[5] = {wt * __uint_as_float(gm[1]), wt * __uint_as_float(g0[0]), wt * __uint_as_float(g0[1]), wt * __uint_as_float(g1[0]), wt * __uint_as_float(g1[1])};
#pragma unroll
      for (int j = 0; j < 5; ++j) { v[j] += dpp<ROR4>(v[j]); v[j] += dpp<XOR8>(v[j]); v[j] = sw16_sum(v[j], v[j]); v[j] = sw32_sum(v[j], v[j]); }
      if (lane < 4) {
        const float inv = 1.0f / v[0];
        f32x4 o4 = {rbf(v[1] * inv), rbf(v[2] * inv), rbf(v[3] * inv), rbf(v[4] * inv)};
        *reinterpret_cast<f32x4*>(&lds[L_OV + 4 * lane]) = o4;
      }
    }
    BAR();
    return;
  }

  const size_t kvbase = ((size_t)h * SK + (SK - W) + 256 * s + 32 * w + r4) * DH + 8 * c16;
  const v4u xa = *reinterpret_cast<const v4u*>(p.x + 8 * lane);
  const v4u xb = *reinterpret_cast<const v4u*>(p.x + 512 + 8 * (lane & 7));
  const int gurow0 = 192 * (hu / 72) + 24 * w;
  const unsigned xq0 = p.x[qrow0 + rs8], xq1 = p.x[qrow0 + 8 + rs8];
  const unsigned xg0 = p.x[gurow0 + rs8], xg1 = p.x[gurow0 + 8 + rs8], xg2 = p.x[gurow0 + 16 + rs8];
  const v4u wq0 = ldw(wqp), wq1 = ldw(wqp + (size_t)8 * NQ);
  const unsigned e0 = *reinterpret_cast<const unsigned*>(p.ws + WS_E);
  const unsigned xfin = p.x[16 * (li < 0 ? 0 : (li > 35 ? 35 : li)) + (tid & 15)];
  PIN();
  BAR();
  PIN();
  v4u kr[8], vr[8], gr[3], ur[3], wor[3], wdr[3];
  const size_t guo = heavy ? (size_t)(gurow0 + rs8) * F + 64 * (hu % 72) + 8 * ch8 : (size_t)0;
  const size_t gust = heavy ? (size_t)8 * F : (size_t)0;
  const int tq = tid < 504 ? tid : 503;
  const int ch72 = tq % 72, rg7 = tq / 72;
#define LD_K0() _Pragma("unroll") for (int i = 0; i < 8; ++i) kr[i] = ldw(p.kc + kvbase + (size_t)(4 * i) * DH);
#define LD_GU0() _Pragma("unroll") for (int i = 0; i < 3; ++i) { gr[i] = ldw(p.wg + guo + gust * i); ur[i] = ldw(p.wu + guo + gust * i); }
#define LD_V() _Pragma("unroll") for (int i = 0; i < 8; ++i) { vr[i] = ldw(p.vc + kvbase + (size_t)(4 * i) * DH); }
#define LD_DO() _Pragma("unroll") for (int i = 0; i < 3; ++i) { const int r = min(rg7 + 7 * i, 17); wdr[i] = ldw(p.wd + (size_t)(18 * c + r) * D + 8 * ch72); } \
                _Pragma("unroll") for (int i = 0; i < 3; ++i) { const int r = min(rg7 + 7 * i, 15); wor[i] = ldw(p.wo + (size_t)(16 * c + r) * D + 8 * ch72); }
  LD_GU0();
  PIN();
  const unsigned tag0 = e0 + 1u, tag1 = ~tag0;
  const LnS lns = ln_stats(lane, xa, xb);
  q_part(lds, lane, w, lns, xq0, xq1, wq0, wq1);
  BAR();
  LD_K0();
  PIN();
  auto gu_partial = [&]() {
  {
    float ag[8] = {0, 0, 0, 0, 0, 0, 0, 0}, au[8] = {0, 0, 0, 0, 0, 0, 0, 0};
    const float xv[3] = {ln_apply(__uint_as_float(xg0 << 16), lns), ln_apply(__uint_as_float(xg1 << 16), lns), ln_apply(__uint_as_float(xg2 << 16), lns)};
#pragma unroll
    for (int i = 0; i < 3; ++i) {
      fma8(ag, xv[i], gr[i]);
      fma8(au, xv[i], ur[i]);
    }
    tr32<4>(ag); tr16<2>(ag); trd<1, XOR8, 8>(ag, lane);
    tr32<4>(au); tr16<2>(au); trd<1, XOR8, 8>(au, lane);
    lds[L_GUP + 128 * w + 8 * ch8 + rs8] = ag[0];
    lds[L_GUP + 128 * w + 64 + 8 * ch8 + rs8] = au[0];
    asm volatile("s_waitcnt lgkmcnt(0)" ::: "memory");
    unsigned old = 0;
    if (lane == 0) old = __hip_atomic_fetch_add(reinterpret_cast<unsigned*>(lds) + L_CNT, 1u, __ATOMIC_RELAXED, __HIP_MEMORY_SCOPE_WORKGROUP);
    old = __builtin_amdgcn_readfirstlane(old);
    if (old == 7u && heavy) {
      asm volatile("" ::: "memory");
      float a = 0.f, b = 0.f;
#pragma unroll
      for (int ww = 0; ww < 8; ++ww) { a += lds[L_GUP + 128 * ww + lane]; b += lds[L_GUP + 128 * ww + 64 + lane]; }
      const v4u g = {__float_as_uint(a), __float_as_uint(b), tag0, tag1};
      __builtin_amdgcn_raw_buffer_store_b128(g, rs, WS_HG + hu * 1024 + lane * 16, 0, 16);
    }
  }
  };
  gu_partial();
  LD_V();
  PIN();
  while (__hip_atomic_load(reinterpret_cast<unsigned*>(lds) + L_CNT + 1, __ATOMIC_RELAXED, __HIP_MEMORY_SCOPE_WORKGROUP) == 0u) __builtin_amdgcn_s_sleep(1);
  asm volatile("" ::: "memory");
  PIN();
  float pv[16];
  {
    v4u qv[2];
#pragma unroll
    for (int g = 0; g < 2; ++g) qv[g] = *reinterpret_cast<const v4u*>(&lds[L_QB + 64 * g + 4 * c16]);
    float x[16];
#pragma unroll
    for (int i = 0; i < 8; ++i)
#pragma unroll
      for (int g = 0; g < 2; ++g) {
        float d = 0.f;
#pragma unroll
        for (int k = 0; k < 4; ++k) d = dot2(kr[i][k], qv[g][k], d);
        x[2 * i + g] = d;
      }
    trd<8, XOR8, 8>(x, lane); trd<4, XOR7, 4>(x, lane); trd<2, XOR2, 2>(x, lane); trd<1, XOR1, 1>(x, lane);
    const float sc = rbf(rbf(x[0]) * 0.08838834764831845f);
    float m = sc;
    m = fmaxf(m, dpp<XOR2>(m)); m = fmaxf(m, dpp<ROR4>(m)); m = fmaxf(m, dpp<XOR8>(m)); m = sw16_max(m); m = sw32_max(m);
    const float pe = __expf(sc - m);
    float l = pe;
    l += dpp<XOR2>(l); l += dpp<ROR4>(l); l += dpp<XOR8>(l); l = sw16_sum(l, l); l = sw32_sum(l, l);
    if (lane < 2) { lds[L_MRG + (w * 2 + lane) * 132 + 128] = m; lds[L_MRG + (w * 2 + lane) * 132 + 129] = l; }
    lds[L_PEX + 64 * w + lane] = pe;
    asm volatile("s_waitcnt lgkmcnt(0)" ::: "memory");
#pragma unroll
    for (int u = 0; u < 4; ++u) {
      const f32x4 t = *reinterpret_cast<const f32x4*>(&lds[L_PEX + 64 * w + 16 * r4 + 4 * u]);
      pv[4 * u] = t[0]; pv[4 * u + 1] = t[1]; pv[4 * u + 2] = t[2]; pv[4 * u + 3] = t[3];
    }
  }
  LD_DO();
  PIN();
  {
    float acc[16];
#pragma unroll
    for (int k = 0; k < 16; ++k) acc[k] = 0.f;
#pragma unroll
    for (int i = 0; i < 8; ++i) {
      fma8(acc, pv[2 * i], vr[i]);
      fma8(acc + 8, pv[2 * i + 1], vr[i]);
    }
    tr32<8>(acc); tr16<4>(acc);
    const f32x4 o4 = {acc[0], acc[1], acc[2], acc[3]};
    *reinterpret_cast<f32x4*>(&lds[L_MRG + (w * 2 + (lane >> 5)) * 132 + 8 * c16 + 4 * ((lane >> 4) & 1)]) = o4;
  }
  BAR();
  BAR();
  if (tid < 504) {
    float acc[8] = {0, 0, 0, 0, 0, 0, 0, 0};
#pragma unroll
    for (int i = 0; i < 3; ++i) {
      const int r = rg7 + 7 * i;
      const float hv = r < 18 ? lds[L_HV + min(r, 17)] : 0.f, ov = r < 16 ? lds[L_OV + min(r, 15)] : 0.f;
      fma8(acc, hv, wdr[i]);
      fma8(acc, ov, wor[i]);
    }
    const f32x4 a0 = {acc[0], acc[1], acc[2], acc[3]}, a1 = {acc[4], acc[5], acc[6], acc[7]};
    *reinterpret_cast<f32x4*>(&lds[L_FIN + 576 * rg7 + 8 * ch72]) = a0;
    *reinterpret_cast<f32x4*>(&lds[L_FIN + 576 * rg7 + 8 * ch72 + 4]) = a1;
  }
  BAR();
  if (tid < 288) {
    float a = 0.f, b = 0.f;
#pragma unroll
    for (int r = 0; r < 7; ++r) { const f32x2 v = *reinterpret_cast<const f32x2*>(&lds[L_FIN + 576 * r + 2 * tid]); a += v[0]; b += v[1]; }
    const v4u g = {__float_as_uint(a), __float_as_uint(b), tag0, tag1};
    __builtin_amdgcn_raw_buffer_store_b128(g, rs, WS_FG + c * 4608 + tid * 16, 0, 16);
  }
  if (heavy || li >= 36) return;
  {
    const int gi = tid & 7, p0 = tid >> 3;
    v4u g[4];
    for (unsigned spin = 0;; ++spin) {
      bool ok = true;
#pragma unroll
      for (int k = 0; k < 4; ++k) g[k] = __builtin_amdgcn_raw_buffer_load_b128(rs, WS_FG + (p0 + 64 * k) * 4608 + li * 128 + gi * 16, 0, 16);
#pragma unroll
      for (int k = 0; k < 4; ++k) ok &= (g[k][2] == tag0) & (g[k][3] == tag1);
      if (__all(ok) || spin > SPIN_CAP) break;
      asm volatile("" ::: "memory");
    }
    float a = 0.f, b = 0.f;
#pragma unroll
    for (int k = 0; k < 4; ++k) { a += __uint_as_float(g[k][0]); b += __uint_as_float(g[k][1]); }
#pragma unroll
    for (int k = 0; k < 1; ++k) { a += dpp<XOR8>(a); b += dpp<XOR8>(b); a = sw16_sum(a, a); b = sw16_sum(b, b); a = sw32_sum(a, a); b = sw32_sum(b, b); }
    if (lane < 8) { lds[L_RED + 16 * w + 2 * lane] = a; lds[L_RED + 16 * w + 2 * lane + 1] = b; }
    BAR();
    if (tid < 16) {
      float t = 0.f;
#pragma unroll
      for (int ww = 0; ww < 8; ++ww) t += lds[L_RED + 16 * ww + tid];
      const int n = 16 * li + tid;
      const float xv = __uint_as_float(xfin << 16);
      p.out[n] = (u16)f2bf(xv + t);
    }
    if (li == 0 && tid == 0) *reinterpret_cast<unsigned*>(p.ws + WS_E) = tag0;
  }
}

extern "C" void kernel_launch(void* const* d_in, const int* in_sizes, int n_in, void* d_out, int out_size, void* d_ws, size_t ws_size,
                              hipStream_t stream) {
  Params p;
  p.x = (const u16*)d_in[0];
  p.kc = (const u16*)d_in[1];
  p.vc = (const u16*)d_in[2];
  p.wq = (const u16*)d_in[3];
  p.wo = (const u16*)d_in[4];
  p.wg = (const u16*)d_in[6];
  p.wu = (const u16*)d_in[7];
  p.wd = (const u16*)d_in[8];
  p.out = (u16*)d_out;
  p.ws = (unsigned char*)d_ws;
  mega_kernel<<<256, NTHR, 0, stream>>>(p);
}
```

```cpp
#include <hip/hip_runtime.h>
#include <hip/hip_bf16.h>
#include <stdint.h>

typedef unsigned v4u __attribute__((ext_vector_type(4)));
typedef float f32x4 __attribute__((ext_vector_type(4)));
typedef float f32x2 __attribute__((ext_vector_type(2)));
typedef unsigned short u16;
using bf16x2 = __attribute__((ext_vector_type(2))) __bf16;

constexpr int D = 576, SK = 32768, DH = 128, W = 4096, F = 4608, NQ = 4096;
constexpr int NTHR = 576;
constexpr int WS_E = 0;
constexpr int WS_QG = 4096;
constexpr int WS_AG = WS_QG + 256 * 512;
constexpr int WS_HG = WS_AG + 256 * 2304;
constexpr int WS_FG = WS_HG + 216 * 1024;
constexpr int WS_END = WS_FG + 256 * 4608;
constexpr unsigned SPIN_CAP = 1u << 16;


struct Params {
  const u16 *x, *kc, *vc, *wq, *wo, *wg, *wu, *wd;
  u16* out;
  unsigned char* ws;
};

__device__ __forceinline__ float rbf(float f) {
  unsigned u = __float_as_uint(f);
  u += 0x7FFFu + ((u >> 16) & 1u);
  return __uint_as_float(u & 0xFFFF0000u);
}
__device__ __forceinline__ unsigned f2bf(float f) {
  unsigned u = __float_as_uint(f);
  u += 0x7FFFu + ((u >> 16) & 1u);
  return u >> 16;
}
__device__ __forceinline__ float lo(unsigned w) { return __uint_as_float(w << 16); }
__device__ __forceinline__ float hi(unsigned w) { return __uint_as_float(w & 0xFFFF0000u); }
__device__ __forceinline__ float dot2(unsigned k, unsigned q, float acc) {
  return __builtin_amdgcn_fdot2_f32_bf16(__builtin_bit_cast(bf16x2, k), __builtin_bit_cast(bf16x2, q), acc, false);
}
__device__ __forceinline__ v4u ldw(const u16* p) { return __builtin_nontemporal_load(reinterpret_cast<const v4u*>(p)); }

template <int CTRL> __device__ __forceinline__ float dpp(float x) {
  return __builtin_bit_cast(float, __builtin_amdgcn_mov_dpp(__builtin_bit_cast(int, x), CTRL, 0xf, 0xf, true));
}
constexpr int XOR1 = 0xB1, XOR2 = 0x4E, XOR7 = 0x141, XOR8 = 0x128, ROR4 = 0x124, MIR = 0x140;
__device__ __forceinline__ float sw32_sum(float a, float b) {
  auto r = __builtin_amdgcn_permlane32_swap(__float_as_uint(a), __float_as_uint(b), false, false);
  return __uint_as_float(r[0]) + __uint_as_float(r[1]);
}
__device__ __forceinline__ float sw16_sum(float a, float b) {
  auto r = __builtin_amdgcn_permlane16_swap(__float_as_uint(a), __float_as_uint(b), false, false);
  return __uint_as_float(r[0]) + __uint_as_float(r[1]);
}
__device__ __forceinline__ float sw32_max(float a) {
  auto r = __builtin_amdgcn_permlane32_swap(__float_as_uint(a), __float_as_uint(a), false, false);
  return fmaxf(__uint_as_float(r[0]), __uint_as_float(r[1]));
}
__device__ __forceinline__ float sw16_max(float a) {
  auto r = __builtin_amdgcn_permlane16_swap(__float_as_uint(a), __float_as_uint(a), false, false);
  return fmaxf(__uint_as_float(r[0]), __uint_as_float(r[1]));
}
__device__ __forceinline__ float wave_sum(float v) {
  v += dpp<XOR1>(v); v += dpp<XOR2>(v); v += dpp<XOR7>(v); v += dpp<XOR8>(v);
  v = sw16_sum(v, v); v = sw32_sum(v, v);
  return v;
}
template <int H> __device__ __forceinline__ void tr32(float* v) {
#pragma unroll
  for (int j = 0; j < H; ++j) v[j] = sw32_sum(v[j], v[j + H]);
}
template <int H> __device__ __forceinline__ void tr16(float* v) {
#pragma unroll
  for (int j = 0; j < H; ++j) v[j] = sw16_sum(v[j], v[j + H]);
}
template <int H, int CTRL, int BIT> __device__ __forceinline__ void trd(float* v, int lane) {
#pragma unroll
  for (int j = 0; j < H; ++j) {
    const float t1 = v[j] + dpp<CTRL>(v[j]), t2 = v[j + H] + dpp<CTRL>(v[j + H]);
    v[j] = (lane & BIT) ? t2 : t1;
  }
}
#define BAR() do { __builtin_amdgcn_sched_barrier(0); asm volatile("s_waitcnt lgkmcnt(0)" ::: "memory"); __builtin_amdgcn_s_barrier(); asm volatile("" ::: "memory"); __builtin_amdgcn_sched_barrier(0); } while (0)
#define PIN() __builtin_amdgcn_sched_barrier(0)

__device__ __forceinline__ void fma8(float* acc, float xv, v4u w) {
#pragma unroll
  for (int k = 0; k < 4; ++k) {
    acc[2 * k] = fmaf(xv, lo(w[k]), acc[2 * k]);
    acc[2 * k + 1] = fmaf(xv, hi(w[k]), acc[2 * k + 1]);
  }
}

constexpr int L_XN = 0;
constexpr int L_QP = L_XN + 576;
constexpr int L_QB = L_QP + 576;
constexpr int L_PEX = L_QB + 128;
constexpr int L_MRG = L_PEX + 512;
constexpr int L_GUP = L_MRG + 8 * 264;
constexpr int L_OV = L_GUP + 1024;
constexpr int L_HV = L_OV + 16;
constexpr int L_FIN = L_HV + 32;
constexpr int L_RED = L_FIN + 7 * 576;
constexpr int L_CNT = L_RED + 160;
constexpr int L_END = L_CNT + 4;

__device__ __forceinline__ float divr(float a, float b, float ib) { const float q = a * ib; const float r = fmaf(-q, b, a); return fmaf(r, ib, q); }
struct LnS { float mb, sd, isd; };
__device__ __forceinline__ LnS ln_stats(int lane, v4u xa, v4u xb) {
  float xv[8], xw[8];
#pragma unroll
  for (int k = 0; k < 4; ++k) { xv[2 * k] = lo(xa[k]); xv[2 * k + 1] = hi(xa[k]); xw[2 * k] = lo(xb[k]); xw[2 * k + 1] = hi(xb[k]); }
  float sm = 0.f, sm2 = 0.f;
#pragma unroll
  for (int k = 0; k < 8; ++k) { sm += xv[k]; sm2 += xw[k]; }
  sm = lane < 8 ? sm + sm2 : sm;
  const float mean = wave_sum(sm) * (1.0f / D);
  float vs = 0.f, vs2 = 0.f;
#pragma unroll
  for (int k = 0; k < 8; ++k) { const float a = xv[k] - mean; vs = fmaf(a, a, vs); const float b = xw[k] - mean; vs2 = fmaf(b, b, vs2); }
  vs = lane < 8 ? vs + vs2 : vs;
  const float var = rbf(wave_sum(vs) * (1.0f / D));
  LnS r;
  r.mb = rbf(mean);
  r.sd = rbf(sqrtf(rbf(var + 1e-5f)));
  r.isd = 1.0f / r.sd;
  return r;
}
__device__ __forceinline__ float ln_apply(float x, const LnS& s) { return rbf(divr(rbf(x - s.mb), s.sd, s.isd)); }
__device__ __forceinline__ void q_part(float* lds, int lane, int w, const LnS& s, unsigned xq0, unsigned xq1, v4u wq0, v4u wq1) {
  float acc[8] = {0, 0, 0, 0, 0, 0, 0, 0};
  fma8(acc, ln_apply(__uint_as_float(xq0 << 16), s), wq0);
  fma8(acc, ln_apply(__uint_as_float(xq1 << 16), s), wq1);
  tr32<4>(acc); tr16<2>(acc); trd<1, XOR8, 8>(acc, lane);
  lds[L_QP + 64 * w + 8 * (lane & 7) + (lane >> 3)] = acc[0];
}
__device__ __forceinline__ void ln_store8(float* dst, v4u xa, const LnS& s) {
  f32x4 o0, o1;
#pragma unroll
  for (int k = 0; k < 2; ++k) { o0[2 * k] = ln_apply(lo(xa[k]), s); o0[2 * k + 1] = ln_apply(hi(xa[k]), s); o1[2 * k] = ln_apply(lo(xa[2 + k]), s); o1[2 * k + 1] = ln_apply(hi(xa[2 + k]), s); }
  *reinterpret_cast<f32x4*>(dst) = o0;
  *reinterpret_cast<f32x4*>(dst + 4) = o1;
}

__global__ void __launch_bounds__(NTHR, 3) mega_kernel(Params p) {
  __shared__ __attribute__((aligned(16))) float lds[L_END];
  const int tid = threadIdx.x, lane = tid & 63;
  const int w = __builtin_amdgcn_readfirstlane(tid >> 6);
  const int c = blockIdx.x;
  const int h = c >> 4, s = c & 15;
  const bool heavy = (c & 31) < 27;
  const int hu = heavy ? (c >> 5) * 27 + (c & 31) : 0;
  const int li = (c >> 5) * 5 + ((c & 31) - 27);
  const __amdgpu_buffer_rsrc_t rs = __builtin_amdgcn_make_buffer_rsrc((void*)p.ws, 0, WS_END, 0x00020000);
  const int ch8 = lane & 7, rs8 = lane >> 3;
  const int r4 = lane >> 4, c16 = lane & 15;
  const int qrow0 = 144 * (s >> 2) + 16 * w;
  const u16* wqp = p.wq + (size_t)(qrow0 + rs8) * NQ + 256 * h + 64 * (s & 3) + 8 * ch8;
  unsigned tlbw = 0;
  if (w == 8) {
      const int gr0 = 192 * (hu / 72);
      const size_t go = (size_t)gr0 * F + 64 * (hu % 72);
      const char* a = (const char*)(p.vc + ((size_t)h * SK + (SK - W) + 256 * s) * DH);
      if (lane == 1) a = (const char*)(p.wg + go);
      if (lane == 2) a = (const char*)(p.wg + go + (size_t)191 * F);
      if (lane == 3) a = (const char*)(p.wu + go);
      if (lane == 4) a = (const char*)(p.wu + go + (size_t)191 * F);
      if (lane == 5) a = (const char*)(p.wd + (size_t)18 * c * D);
      if (lane == 6) a = (const char*)(p.wo + (size_t)16 * c * D);
      if (lane == 7) a = (const char*)(p.out);
      if (lane == 8) a = (const char*)(p.ws + WS_FG + c * 4608);
      if (lane == 9) a = (const char*)(p.ws + WS_AG + c * 2304);
      if (lane == 10) a = (const char*)(p.ws + WS_HG + hu * 1024);
      if (lane == 11) a = (const char*)(p.kc + ((size_t)h * SK + (SK - W) + 256 * s) * DH);
      if (lane == 12) a = (const char*)(p.x);
      if (lane == 13) a = (const char*)(p.wq + (size_t)(144 * (s >> 2)) * NQ + 256 * h);
      if (lane == 14) a = (const char*)(p.wq + (size_t)(144 * (s >> 2) + 143) * NQ + 256 * h);
      if (lane == 15) a = (const char*)(p.ws);
      tlbw = *reinterpret_cast<const unsigned*>(a);
      asm volatile("" :: "v"(tlbw));
  }
  { const unsigned long long t0 = __builtin_amdgcn_s_memrealtime(); while (__builtin_amdgcn_s_memrealtime() - t0 < 600ull) __builtin_amdgcn_s_sleep(2); }

  if (w == 8) {
    __builtin_amdgcn_s_setprio(3);
    const v4u xa = *reinterpret_cast<const v4u*>(p.x + 8 * lane);
    const v4u xb = *reinterpret_cast<const v4u*>(p.x + 512 + 8 * (lane & 7));
    const unsigned xq0 = p.x[qrow0 + rs8], xq1 = p.x[qrow0 + 8 + rs8];
    const v4u wq0 = ldw(wqp), wq1 = ldw(wqp + (size_t)8 * NQ);
    const unsigned e0 = *reinterpret_cast<const unsigned*>(p.ws + WS_E);
    if (lane < 4) reinterpret_cast<unsigned*>(lds)[L_CNT + lane] = 0u;
    PIN();
    BAR();
    PIN();
    const unsigned tag0 = e0 + 1u, tag1 = ~tag0;
    const LnS lns = ln_stats(lane, xa, xb);
    q_part(lds, lane, w, lns, xq0, xq1, wq0, wq1);
    BAR();
    {
      if (lane < 32) {
        float a = 0.f, b = 0.f;
#pragma unroll
        for (int ww = 0; ww < 9; ++ww) { const f32x2 v = *reinterpret_cast<const f32x2*>(&lds[L_QP + 64 * ww + 2 * lane]); a += v[0]; b += v[1]; }
        const v4u g = {__float_as_uint(a), __float_as_uint(b), tag0, tag1};
        __builtin_amdgcn_raw_buffer_store_b128(g, rs, WS_QG + c * 512 + lane * 16, 0, 16);
      }
    }
    {
      v4u g[8];
      for (unsigned spin = 0;; ++spin) {
        bool ok = true;
#pragma unroll
        for (int k = 0; k < 8; ++k) g[k] = __builtin_amdgcn_raw_buffer_load_b128(rs, WS_QG + (16 * h + (lane >> 5) + 2 * k) * 512 + (lane & 31) * 16, 0, 16);
#pragma unroll
        for (int k = 0; k < 8; ++k) ok &= (g[k][2] == tag0) & (g[k][3] == tag1);
        if (__all(ok) || spin > SPIN_CAP) break;
        asm volatile("" ::: "memory");
      }
      float a[2] = {0.f, 0.f}, b[2] = {0.f, 0.f};
#pragma unroll
      for (int k = 0; k < 8; ++k) { a[k & 1] += __uint_as_float(g[k][0]); b[k & 1] += __uint_as_float(g[k][1]); }
#pragma unroll
      for (int j = 0; j < 2; ++j) {
        const int cg = (lane >> 5) + 2 * j;
        reinterpret_cast<unsigned*>(lds)[L_QB + 32 * cg + (lane & 31)] = f2bf(a[j]) | (f2bf(b[j]) << 16);
      }
    }
    asm volatile("s_waitcnt lgkmcnt(0)" ::: "memory");
    if (lane == 0) __hip_atomic_store(reinterpret_cast<unsigned*>(lds) + L_CNT + 1, 1u, __ATOMIC_RELAXED, __HIP_MEMORY_SCOPE_WORKGROUP);
    BAR();
    {
#pragma unroll
      for (int g = 0; g < 2; ++g) {
        float mw[8], M = -INFINITY;
#pragma unroll
        for (int ww = 0; ww < 8; ++ww) { mw[ww] = lds[L_MRG + (ww * 2 + g) * 132 + 128]; M = fmaxf(M, mw[ww]); }
        float L = 0.f, a = 0.f, b = 0.f;
#pragma unroll
        for (int ww = 0; ww < 8; ++ww) {
          const float wt = __expf(mw[ww] - M);
          L = fmaf(wt, lds[L_MRG + (ww * 2 + g) * 132 + 129], L);
          const f32x2 v = *reinterpret_cast<const f32x2*>(&lds[L_MRG + (ww * 2 + g) * 132 + 2 * lane]);
          a = fmaf(wt, v[0], a); b = fmaf(wt, v[1], b);
        }
        const int rec = WS_AG + c * 2304 + (g * 8 + (lane >> 3)) * 144;
        const v4u ga = {__float_as_uint(a), __float_as_uint(b), tag0, tag1};
        __builtin_amdgcn_raw_buffer_store_b128(ga, rs, rec + 16 + (lane & 7) * 16, 0, 16);
        if ((lane & 7) == 0) {
          const v4u gm = {__float_as_uint(M), __float_as_uint(L), tag0, tag1};
          __builtin_amdgcn_raw_buffer_store_b128(gm, rs, rec, 0, 16);
        }
      }
    }
    {
      const int pr = lane >> 2, q4 = lane & 3;
      const int rec = WS_AG + (16 * h + pr) * 2304 + s * 144;
      const int ks = lane / 18, j = lane - 18 * ks;
      const int f = 18 * c + j;
      const int hoff = WS_HG + ((ks < 3 ? ks : 0) * 72 + (f >> 6)) * 1024 + (f & 63) * 16;
      v4u gm, g0, g1, gh;
      for (unsigned spin = 0;; ++spin) {
        gm = __builtin_amdgcn_raw_buffer_load_b128(rs, rec, 0, 16);
        g0 = __builtin_amdgcn_raw_buffer_load_b128(rs, rec + 16 + 32 * q4, 0, 16);
        g1 = __builtin_amdgcn_raw_buffer_load_b128(rs, rec + 32 + 32 * q4, 0, 16);
        gh = __builtin_amdgcn_raw_buffer_load_b128(rs, hoff, 0, 16);
        const bool ok = (gm[2] == tag0) & (gm[3] == tag1) & (g0[2] == tag0) & (g0[3] == tag1) & (g1[2] == tag0) & (g1[3] == tag1) & (gh[2] == tag0) & (gh[3] == tag1);
        if (__all(ok) || spin > SPIN_CAP) break;
        asm volatile("" ::: "memory");
      }
      {
        const float a = __uint_as_float(gh[0]), b = __uint_as_float(gh[1]);
        const float a1 = __shfl(a, lane + 18), b1 = __shfl(b, lane + 18), a2 = __shfl(a, lane + 36), b2 = __shfl(b, lane + 36);
        if (lane < 18) {
          float gt = rbf(a + a1 + a2);
          const float up = rbf(b + b1 + b2);
          gt = gt > 0.f ? gt : 0.f;
          lds[L_HV + lane] = rbf(gt * up);
        }
      }
      const float m = __uint_as_float(gm[0]);
      float M = m;
      M = fmaxf(M, dpp<ROR4>(M)); M = fmaxf(M, dpp<XOR8>(M)); M = sw16_max(M); M = sw32_max(M);
      const float wt = __expf(m - M);
      float v[5] = {wt * __uint_as_float(gm[1]), wt * __uint_as_float(g0[0]), wt * __uint_as_float(g0[1]), wt * __uint_as_float(g1[0]), wt * __uint_as_float(g1[1])};
#pragma unroll
      for (int j = 0; j < 5; ++j) { v[j] += dpp<ROR4>(v[j]); v[j] += dpp<XOR8>(v[j]); v[j] = sw16_sum(v[j], v[j]); v[j] = sw32_sum(v[j], v[j]); }
      if (lane < 4) {
        const float inv = 1.0f / v[0];
        f32x4 o4 = {rbf(v[1] * inv), rbf(v[2] * inv), rbf(v[3] * inv), rbf(v[4] * inv)};
        *reinterpret_cast<f32x4*>(&lds[L_OV + 4 * lane]) = o4;
      }
    }
    BAR();
    return;
  }

  const size_t kvbase = ((size_t)h * SK + (SK - W) + 256 * s + 32 * w + r4) * DH + 8 * c16;
  const v4u xa = *reinterpret_cast<const v4u*>(p.x + 8 * lane);
  const v4u xb = *reinterpret_cast<const v4u*>(p.x + 512 + 8 * (lane & 7));
  const int gurow0 = 192 * (hu / 72) + 24 * w;
  const unsigned xq0 = p.x[qrow0 + rs8], xq1 = p.x[qrow0 + 8 + rs8];
  const unsigned xg0 = p.x[gurow0 + rs8], xg1 = p.x[gurow0 + 8 + rs8], xg2 = p.x[gurow0 + 16 + rs8];
  const v4u wq0 = ldw(wqp), wq1 = ldw(wqp + (size_t)8 * NQ);
  const unsigned e0 = *reinterpret_cast<const unsigned*>(p.ws + WS_E);
  const unsigned xfin = p.x[16 * (li < 0 ? 0 : (li > 35 ? 35 : li)) + (tid & 15)];
  PIN();
  BAR();
  PIN();
  v4u kr[8], vr[8], gr[3], ur[3], wor[3], wdr[3];
  const size_t guo = heavy ? (size_t)(gurow0 + rs8) * F + 64 * (hu % 72) + 8 * ch8 : (size_t)0;
  const size_t gust = heavy ? (size_t)8 * F : (size_t)0;
  const int tq = tid < 504 ? tid : 503;
  const int ch72 = tq % 72, rg7 = tq / 72;
#define LD_K0() _Pragma("unroll") for (int i = 0; i < 8; ++i) kr[i] = ldw(p.kc + kvbase + (size_t)(4 * i) * DH);
#define LD_GU0() _Pragma("unroll") for (int i = 0; i < 3; ++i) { gr[i] = ldw(p.wg + guo + gust * i); ur[i] = ldw(p.wu + guo + gust * i); }
#define LD_V() _Pragma("unroll") for (int i = 0; i < 8; ++i) { vr[i] = ldw(p.vc + kvbase + (size_t)(4 * i) * DH); }
#define LD_DO() _Pragma("unroll") for (int i = 0; i < 3; ++i) { const int r = min(rg7 + 7 * i, 17); wdr[i] = ldw(p.wd + (size_t)(18 * c + r) * D + 8 * ch72); } \
                _Pragma("unroll") for (int i = 0; i < 3; ++i) { const int r = min(rg7 + 7 * i, 15); wor[i] = ldw(p.wo + (size_t)(16 * c + r) * D + 8 * ch72); }
  LD_GU0();
  PIN();
  const unsigned tag0 = e0 + 1u, tag1 = ~tag0;
  const LnS lns = ln_stats(lane, xa, xb);
  q_part(lds, lane, w, lns, xq0, xq1, wq0, wq1);
  BAR();
  LD_K0();
  PIN();
  auto gu_partial = [&]() {
  {
    float ag[8] = {0, 0, 0, 0, 0, 0, 0, 0}, au[8] = {0, 0, 0, 0, 0, 0, 0, 0};
    const float xv[3] = {ln_apply(__uint_as_float(xg0 << 16), lns), ln_apply(__uint_as_float(xg1 << 16), lns), ln_apply(__uint_as_float(xg2 << 16), lns)};
#pragma unroll
    for (int i = 0; i < 3; ++i) {
      fma8(ag, xv[i], gr[i]);
      fma8(au, xv[i], ur[i]);
    }
    tr32<4>(ag); tr16<2>(ag); trd<1, XOR8, 8>(ag, lane);
    tr32<4>(au); tr16<2>(au); trd<1, XOR8, 8>(au, lane);
    lds[L_GUP + 128 * w + 8 * ch8 + rs8] = ag[0];
    lds[L_GUP + 128 * w + 64 + 8 * ch8 + rs8] = au[0];
    asm volatile("s_waitcnt lgkmcnt(0)" ::: "memory");
    unsigned old = 0;
    if (lane == 0) old = __hip_atomic_fetch_add(reinterpret_cast<unsigned*>(lds) + L_CNT, 1u, __ATOMIC_RELAXED, __HIP_MEMORY_SCOPE_WORKGROUP);
    old = __builtin_amdgcn_readfirstlane(old);
    if (old == 7u && heavy) {
      asm volatile("" ::: "memory");
      float a = 0.f, b = 0.f;
#pragma unroll
      for (int ww = 0; ww < 8; ++ww) { a += lds[L_GUP + 128 * ww + lane]; b += lds[L_GUP + 128 * ww + 64 + lane]; }
      const v4u g = {__float_as_uint(a), __float_as_uint(b), tag0, tag1};
      __builtin_amdgcn_raw_buffer_store_b128(g, rs, WS_HG + hu * 1024 + lane * 16, 0, 16);
    }
  }
  };
  gu_partial();
  LD_V();
  PIN();
  while (__hip_atomic_load(reinterpret_cast<unsigned*>(lds) + L_CNT + 1, __ATOMIC_RELAXED, __HIP_MEMORY_SCOPE_WORKGROUP) == 0u) __builtin_amdgcn_s_sleep(1);
  asm volatile("" ::: "memory");
  PIN();
  float pv[16];
  {
    v4u qv[2];
#pragma unroll
    for (int g = 0; g < 2; ++g) qv[g] = *reinterpret_cast<const v4u*>(&lds[L_QB + 64 * g + 4 * c16]);
    float x[16];
#pragma unroll
    for (int i = 0; i < 8; ++i)
#pragma unroll
      for (int g = 0; g < 2; ++g) {
        float d = 0.f;
#pragma unroll
        for (int k = 0; k < 4; ++k) d = dot2(kr[i][k], qv[g][k], d);
        x[2 * i + g] = d;
      }
    trd<8, XOR8, 8>(x, lane); trd<4, XOR7, 4>(x, lane); trd<2, XOR2, 2>(x, lane); trd<1, XOR1, 1>(x, lane);
    const float sc = rbf(rbf(x[0]) * 0.08838834764831845f);
    float m = sc;
    m = fmaxf(m, dpp<XOR2>(m)); m = fmaxf(m, dpp<ROR4>(m)); m = fmaxf(m, dpp<XOR8>(m)); m = sw16_max(m); m = sw32_max(m);
    const float pe = __expf(sc - m);
    float l = pe;
    l += dpp<XOR2>(l); l += dpp<ROR4>(l); l += dpp<XOR8>(l); l = sw16_sum(l, l); l = sw32_sum(l, l);
    if (lane < 2) { lds[L_MRG + (w * 2 + lane) * 132 + 128] = m; lds[L_MRG + (w * 2 + lane) * 132 + 129] = l; }
    lds[L_PEX + 64 * w + lane] = pe;
    asm volatile("s_waitcnt lgkmcnt(0)" ::: "memory");
#pragma unroll
    for (int u = 0; u < 4; ++u) {
      const f32x4 t = *reinterpret_cast<const f32x4*>(&lds[L_PEX + 64 * w + 16 * r4 + 4 * u]);
      pv[4 * u] = t[0]; pv[4 * u + 1] = t[1]; pv[4 * u + 2] = t[2]; pv[4 * u + 3] = t[3];
    }
  }
  LD_DO();
  PIN();
  {
    float acc[16];
#pragma unroll
    for (int k = 0; k < 16; ++k) acc[k] = 0.f;
#pragma unroll
    for (int i = 0; i < 8; ++i) {
      fma8(acc, pv[2 * i], vr[i]);
      fma8(acc + 8, pv[2 * i + 1], vr[i]);
    }
    tr32<8>(acc); tr16<4>(acc);
    const f32x4 o4 = {acc[0], acc[1], acc[2], acc[3]};
    *reinterpret_cast<f32x4*>(&lds[L_MRG + (w * 2 + (lane >> 5)) * 132 + 8 * c16 + 4 * ((lane >> 4) & 1)]) = o4;
  }
  BAR();
  BAR();
  if (tid < 504) {
    float acc[8] = {0, 0, 0, 0, 0, 0, 0, 0};
#pragma unroll
    for (int i = 0; i < 3; ++i) {
      const int r = rg7 + 7 * i;
      const float hv = r < 18 ? lds[L_HV + min(r, 17)] : 0.f, ov = r < 16 ? lds[L_OV + min(r, 15)] : 0.f;
      fma8(acc, hv, wdr[i]);
      fma8(acc, ov, wor[i]);
    }
    const f32x4 a0 = {acc[0], acc[1], acc[2], acc[3]}, a1 = {acc[4], acc[5], acc[6], acc[7]};
    *reinterpret_cast<f32x4*>(&lds[L_FIN + 576 * rg7 + 8 * ch72]) = a0;
    *reinterpret_cast<f32x4*>(&lds[L_FIN + 576 * rg7 + 8 * ch72 + 4]) = a1;
  }
  BAR();
  if (tid < 288) {
    float a = 0.f, b = 0.f;
#pragma unroll
    for (int r = 0; r < 7; ++r) { const f32x2 v = *reinterpret_cast<const f32x2*>(&lds[L_FIN + 576 * r + 2 * tid]); a += v[0]; b += v[1]; }
    const v4u g = {__float_as_uint(a), __float_as_uint(b), tag0, tag1};
    __builtin_amdgcn_raw_buffer_store_b128(g, rs, WS_FG + c * 4608 + tid * 16, 0, 16);
  }
  if (heavy || li >= 36) return;
  {
    const int gi = tid & 7, p0 = tid >> 3;
    v4u g[4];
    for (unsigned spin = 0;; ++spin) {
      bool ok = true;
#pragma unroll
      for (int k = 0; k < 4; ++k) g[k] = __builtin_amdgcn_raw_buffer_load_b128(rs, WS_FG + (p0 + 64 * k) * 4608 + li * 128 + gi * 16, 0, 16);
#pragma unroll
      for (int k = 0; k < 4; ++k) ok &= (g[k][2] == tag0) & (g[k][3] == tag1);
      if (__all(ok) || spin > SPIN_CAP) break;
      asm volatile("" ::: "memory");
    }
    float a = 0.f, b = 0.f;
#pragma unroll
    for (int k = 0; k < 4; ++k) { a += __uint_as_float(g[k][0]); b += __uint_as_float(g[k][1]); }
#pragma unroll
    for (int k = 0; k < 1; ++k) { a += dpp<XOR8>(a); b += dpp<XOR8>(b); a = sw16_sum(a, a); b = sw16_sum(b, b); a = sw32_sum(a, a); b = sw32_sum(b, b); }
    if (lane < 8) { lds[L_RED + 16 * w + 2 * lane] = a; lds[L_RED + 16 * w + 2 * lane + 1] = b; }
    BAR();
    if (tid < 16) {
      float t = 0.f;
#pragma unroll
      for (int ww = 0; ww < 8; ++ww) t += lds[L_RED + 16 * ww + tid];
      const int n = 16 * li + tid;
      const float xv = __uint_as_float(xfin << 16);
      p.out[n] = (u16)f2bf(xv + t);
    }
    if (li == 0 && tid == 0) *reinterpret_cast<unsigned*>(p.ws + WS_E) = tag0;
  }
}

extern "C" void kernel_launch(void* const* d_in, const int* in_sizes, int n_in, void* d_out, int out_size, void* d_ws, size_t ws_size,
                              hipStream_t stream) {
  Params p;
  p.x = (const u16*)d_in[0];
  p.kc = (const u16*)d_in[1];
  p.vc = (const u16*)d_in[2];
  p.wq = (const u16*)d_in[3];
  p.wo = (const u16*)d_in[4];
  p.wg = (const u16*)d_in[6];
  p.wu = (const u16*)d_in[7];
  p.wd = (const u16*)d_in[8];
  p.out = (u16*)d_out;
  p.ws = (unsigned char*)d_ws;
  mega_kernel<<<256, NTHR, 0, stream>>>(p);
}
```

```cpp
#include <hip/hip_runtime.h>
#include <hip/hip_bf16.h>
#include <stdint.h>

typedef unsigned v4u __attribute__((ext_vector_type(4)));
typedef float f32x4 __attribute__((ext_vector_type(4)));
typedef float f32x2 __attribute__((ext_vector_type(2)));
typedef unsigned short u16;
using bf16x2 = __attribute__((ext_vector_type(2))) __bf16;

constexpr int D = 576, SK = 32768, DH = 128, W = 4096, F = 4608, NQ = 4096;
constexpr int NTHR = 576;
constexpr int WS_E = 0;
constexpr int WS_QG = 4096;
constexpr int WS_AG = WS_QG + 256 * 512;
constexpr int WS_HG = WS_AG + 256 * 2304;
constexpr int WS_FG = WS_HG + 216 * 1024;
constexpr int WS_END = WS_FG + 256 * 4608;
constexpr unsigned SPIN_CAP = 1u << 16;


struct Params {
  const u16 *x, *kc, *vc, *wq, *wo, *wg, *wu, *wd;
  u16* out;
  unsigned char* ws;
};

__device__ __forceinline__ float rbf(float f) {
  unsigned u = __float_as_uint(f);
  u += 0x7FFFu + ((u >> 16) & 1u);
  return __uint_as_float(u & 0xFFFF0000u);
}
__device__ __forceinline__ unsigned f2bf(float f) {
  unsigned u = __float_as_uint(f);
  u += 0x7FFFu + ((u >> 16) & 1u);
  return u >> 16;
}
__device__ __forceinline__ float lo(unsigned w) { return __uint_as_float(w << 16); }
__device__ __forceinline__ float hi(unsigned w) { return __uint_as_float(w & 0xFFFF0000u); }
__device__ __forceinline__ float dot2(unsigned k, unsigned q, float acc) {
  return __builtin_amdgcn_fdot2_f32_bf16(__builtin_bit_cast(bf16x2, k), __builtin_bit_cast(bf16x2, q), acc, false);
}
__device__ __forceinline__ v4u ldw(const u16* p) { return __builtin_nontemporal_load(reinterpret_cast<const v4u*>(p)); }

template <int CTRL> __device__ __forceinline__ float dpp(float x) {
  return __builtin_bit_cast(float, __builtin_amdgcn_mov_dpp(__builtin_bit_cast(int, x), CTRL, 0xf, 0xf, true));
}
constexpr int XOR1 = 0xB1, XOR2 = 0x4E, XOR7 = 0x141, XOR8 = 0x128, ROR4 = 0x124, MIR = 0x140;
__device__ __forceinline__ float sw32_sum(float a, float b) {
  auto r = __builtin_amdgcn_permlane32_swap(__float_as_uint(a), __float_as_uint(b), false, false);
  return __uint_as_float(r[0]) + __uint_as_float(r[1]);
}
__device__ __forceinline__ float sw16_sum(float a, float b) {
  auto r = __builtin_amdgcn_permlane16_swap(__float_as_uint(a), __float_as_uint(b), false, false);
  return __uint_as_float(r[0]) + __uint_as_float(r[1]);
}
__device__ __forceinline__ float sw32_max(float a) {
  auto r = __builtin_amdgcn_permlane32_swap(__float_as_uint(a), __float_as_uint(a), false, false);
  return fmaxf(__uint_as_float(r[0]), __uint_as_float(r[1]));
}
__device__ __forceinline__ float sw16_max(float a) {
  auto r = __builtin_amdgcn_permlane16_swap(__float_as_uint(a), __float_as_uint(a), false, false);
  return fmaxf(__uint_as_float(r[0]), __uint_as_float(r[1]));
}
__device__ __forceinline__ float wave_sum(float v) {
  v += dpp<XOR1>(v); v += dpp<XOR2>(v); v += dpp<XOR7>(v); v += dpp<XOR8>(v);
  v = sw16_sum(v, v); v = sw32_sum(v, v);
  return v;
}
template <int H> __device__ __forceinline__ void tr32(float* v) {
#pragma unroll
  for (int j = 0; j < H; ++j) v[j] = sw32_sum(v[j], v[j + H]);
}
template <int H> __device__ __forceinline__ void tr16(float* v) {
#pragma unroll
  for (int j = 0; j < H; ++j) v[j] = sw16_sum(v[j], v[j + H]);
}
template <int H, int CTRL, int BIT> __device__ __forceinline__ void trd(float* v, int lane) {
#pragma unroll
  for (int j = 0; j < H; ++j) {
    const float t1 = v[j] + dpp<CTRL>(v[j]), t2 = v[j + H] + dpp<CTRL>(v[j + H]);
    v[j] = (lane & BIT) ? t2 : t1;
  }
}
#define BAR() do { __builtin_amdgcn_sched_barrier(0); asm volatile("s_waitcnt lgkmcnt(0)" ::: "memory"); __builtin_amdgcn_s_barrier(); asm volatile("" ::: "memory"); __builtin_amdgcn_sched_barrier(0); } while (0)
#define PIN() __builtin_amdgcn_sched_barrier(0)

__device__ __forceinline__ void fma8(float* acc, float xv, v4u w) {
#pragma unroll
  for (int k = 0; k < 4; ++k) {
    acc[2 * k] = fmaf(xv, lo(w[k]), acc[2 * k]);
    acc[2 * k + 1] = fmaf(xv, hi(w[k]), acc[2 * k + 1]);
  }
}

constexpr int L_XN = 0;
constexpr int L_QP = L_XN + 576;
constexpr int L_QB = L_QP + 576;
constexpr int L_PEX = L_QB + 128;
constexpr int L_MRG = L_PEX + 512;
constexpr int L_GUP = L_MRG + 8 * 264;
constexpr int L_OV = L_GUP + 1024;
constexpr int L_HV = L_OV + 16;
constexpr int L_FIN = L_HV + 32;
constexpr int L_RED = L_FIN + 7 * 576;
constexpr int L_CNT = L_RED + 160;
constexpr int L_END = L_CNT + 4;

__device__ __forceinline__ float divr(float a, float b, float ib) { const float q = a * ib; const float r = fmaf(-q, b, a); return fmaf(r, ib, q); }
struct LnS { float mb, sd, isd; };
__device__ __forceinline__ LnS ln_stats(int lane, v4u xa, v4u xb) {
  float xv[8], xw[8];
#pragma unroll
  for (int k = 0; k < 4; ++k) { xv[2 * k] = lo(xa[k]); xv[2 * k + 1] = hi(xa[k]); xw[2 * k] = lo(xb[k]); xw[2 * k + 1] = hi(xb[k]); }
  float sm = 0.f, sm2 = 0.f;
#pragma unroll
  for (int k = 0; k < 8; ++k) { sm += xv[k]; sm2 += xw[k]; }
  sm = lane < 8 ? sm + sm2 : sm;
  const float mean = wave_sum(sm) * (1.0f / D);
  float vs = 0.f, vs2 = 0.f;
#pragma unroll
  for (int k = 0; k < 8; ++k) { const float a = xv[k] - mean; vs = fmaf(a, a, vs); const float b = xw[k] - mean; vs2 = fmaf(b, b, vs2); }
  vs = lane < 8 ? vs + vs2 : vs;
  const float var = rbf(wave_sum(vs) * (1.0f / D));
  LnS r;
  r.mb = rbf(mean);
  r.sd = rbf(sqrtf(rbf(var + 1e-5f)));
  r.isd = 1.0f / r.sd;
  return r;
}
__device__ __forceinline__ float ln_apply(float x, const LnS& s) { return rbf(divr(rbf(x - s.mb), s.sd, s.isd)); }
__device__ __forceinline__ void q_part(float* lds, int lane, int w, const LnS& s, unsigned xq0, unsigned xq1, v4u wq0, v4u wq1) {
  float acc[8] = {0, 0, 0, 0, 0, 0, 0, 0};
  fma8(acc, ln_apply(__uint_as_float(xq0 << 16), s), wq0);
  fma8(acc, ln_apply(__uint_as_float(xq1 << 16), s), wq1);
  tr32<4>(acc); tr16<2>(acc); trd<1, XOR8, 8>(acc, lane);
  lds[L_QP + 64 * w + 8 * (lane & 7) + (lane >> 3)] = acc[0];
}
__device__ __forceinline__ void ln_store8(float* dst, v4u xa, const LnS& s) {
  f32x4 o0, o1;
#pragma unroll
  for (int k = 0; k < 2; ++k) { o0[2 * k] = ln_apply(lo(xa[k]), s); o0[2 * k + 1] = ln_apply(hi(xa[k]), s); o1[2 * k] = ln_apply(lo(xa[2 + k]), s); o1[2 * k + 1] = ln_apply(hi(xa[2 + k]), s); }
  *reinterpret_cast<f32x4*>(dst) = o0;
  *reinterpret_cast<f32x4*>(dst + 4) = o1;
}

__global__ void __launch_bounds__(NTHR, 3) mega_kernel(Params p) {
  __shared__ __attribute__((aligned(16))) float lds[L_END];
  const int tid = threadIdx.x, lane = tid & 63;
  const int w = __builtin_amdgcn_readfirstlane(tid >> 6);
  const int c = blockIdx.x;
  const int h = c >> 4, s = c & 15;
  const bool heavy = (c & 31) < 27;
  const int hu = heavy ? (c >> 5) * 27 + (c & 31) : 0;
  const int li = (c >> 5) * 5 + ((c & 31) - 27);
  const __amdgpu_buffer_rsrc_t rs = __builtin_amdgcn_make_buffer_rsrc((void*)p.ws, 0, WS_END, 0x00020000);
  const int ch8 = lane & 7, rs8 = lane >> 3;
  const int r4 = lane >> 4, c16 = lane & 15;
  const int qrow0 = 144 * (s >> 2) + 16 * w;
  const u16* wqp = p.wq + (size_t)(qrow0 + rs8) * NQ + 256 * h + 64 * (s & 3) + 8 * ch8;

  if (w == 8) {
    __builtin_amdgcn_s_setprio(3);
    const v4u xa = *reinterpret_cast<const v4u*>(p.x + 8 * lane);
    const v4u xb = *reinterpret_cast<const v4u*>(p.x + 512 + 8 * (lane & 7));
    const unsigned xq0 = p.x[qrow0 + rs8], xq1 = p.x[qrow0 + 8 + rs8];
    const v4u wq0 = ldw(wqp), wq1 = ldw(wqp + (size_t)8 * NQ);
    const unsigned e0 = *reinterpret_cast<const unsigned*>(p.ws + WS_E);
    if (lane < 4) reinterpret_cast<unsigned*>(lds)[L_CNT + lane] = 0u;
    PIN();
    BAR();
    PIN();
    const unsigned tag0 = e0 + 1u, tag1 = ~tag0;
    const LnS lns = ln_stats(lane, xa, xb);
    q_part(lds, lane, w, lns, xq0, xq1, wq0, wq1);
    BAR();
    {
      if (lane < 32) {
        float a = 0.f, b = 0.f;
#pragma unroll
        for (int ww = 0; ww < 9; ++ww) { const f32x2 v = *reinterpret_cast<const f32x2*>(&lds[L_QP + 64 * ww + 2 * lane]); a += v[0]; b += v[1]; }
        const v4u g = {__float_as_uint(a), __float_as_uint(b), tag0, tag1};
        __builtin_amdgcn_raw_buffer_store_b128(g, rs, WS_QG + c * 512 + lane * 16, 0, 16);
      }
    }
    {
      v4u g[8];
      for (unsigned spin = 0;; ++spin) {
        bool ok = true;
#pragma unroll
        for (int k = 0; k < 8; ++k) g[k] = __builtin_amdgcn_raw_buffer_load_b128(rs, WS_QG + (16 * h + (lane >> 5) + 2 * k) * 512 + (lane & 31) * 16, 0, 16);
#pragma unroll
        for (int k = 0; k < 8; ++k) ok &= (g[k][2] == tag0) & (g[k][3] == tag1);
        if (__all(ok) || spin > SPIN_CAP) break;
        asm volatile("" ::: "memory");
      }
      float a[2] = {0.f, 0.f}, b[2] = {0.f, 0.f};
#pragma unroll
      for (int k = 0; k < 8; ++k) { a[k & 1] += __uint_as_float(g[k][0]); b[k & 1] += __uint_as_float(g[k][1]); }
#pragma unroll
      for (int j = 0; j < 2; ++j) {
        const int cg = (lane >> 5) + 2 * j;
        reinterpret_cast<unsigned*>(lds)[L_QB + 32 * cg + (lane & 31)] = f2bf(a[j]) | (f2bf(b[j]) << 16);
      }
    }
    asm volatile("s_waitcnt lgkmcnt(0)" ::: "memory");
    if (lane == 0) __hip_atomic_store(reinterpret_cast<unsigned*>(lds) + L_CNT + 1, 1u, __ATOMIC_RELAXED, __HIP_MEMORY_SCOPE_WORKGROUP);
    BAR();
    {
#pragma unroll
      for (int g = 0; g < 2; ++g) {
        float mw[8], M = -INFINITY;
#pragma unroll
        for (int ww = 0; ww < 8; ++ww) { mw[ww] = lds[L_MRG + (ww * 2 + g) * 132 + 128]; M = fmaxf(M, mw[ww]); }
        float L = 0.f, a = 0.f, b = 0.f;
#pragma unroll
        for (int ww = 0; ww < 8; ++ww) {
          const float wt = __expf(mw[ww] - M);
          L = fmaf(wt, lds[L_MRG + (ww * 2 + g) * 132 + 129], L);
          const f32x2 v = *reinterpret_cast<const f32x2*>(&lds[L_MRG + (ww * 2 + g) * 132 + 2 * lane]);
          a = fmaf(wt, v[0], a); b = fmaf(wt, v[1], b);
        }
        const int rec = WS_AG + c * 2304 + (g * 8 + (lane >> 3)) * 144;
        const v4u ga = {__float_as_uint(a), __float_as_uint(b), tag0, tag1};
        __builtin_amdgcn_raw_buffer_store_b128(ga, rs, rec + 16 + (lane & 7) * 16, 0, 16);
        if ((lane & 7) == 0) {
          const v4u gm = {__float_as_uint(M), __float_as_uint(L), tag0, tag1};
          __builtin_amdgcn_raw_buffer_store_b128(gm, rs, rec, 0, 16);
        }
      }
    }
    {
      const int pr = lane >> 2, q4 = lane & 3;
      const int rec = WS_AG + (16 * h + pr) * 2304 + s * 144;
      const int ks = lane / 18, j = lane - 18 * ks;
      const int f = 18 * c + j;
      const int hoff = WS_HG + ((ks < 3 ? ks : 0) * 72 + (f >> 6)) * 1024 + (f & 63) * 16;
      v4u gm, g0, g1, gh;
      for (unsigned spin = 0;; ++spin) {
        gm = __builtin_amdgcn_raw_buffer_load_b128(rs, rec, 0, 16);
        g0 = __builtin_amdgcn_raw_buffer_load_b128(rs, rec + 16 + 32 * q4, 0, 16);
        g1 = __builtin_amdgcn_raw_buffer_load_b128(rs, rec + 32 + 32 * q4, 0, 16);
        gh = __builtin_amdgcn_raw_buffer_load_b128(rs, hoff, 0, 16);
        const bool ok = (gm[2] == tag0) & (gm[3] == tag1) & (g0[2] == tag0) & (g0[3] == tag1) & (g1[2] == tag0) & (g1[3] == tag1) & (gh[2] == tag0) & (gh[3] == tag1);
        if (__all(ok) || spin > SPIN_CAP) break;
        asm volatile("" ::: "memory");
      }
      {
        const float a = __uint_as_float(gh[0]), b = __uint_as_float(gh[1]);
        const float a1 = __shfl(a, lane + 18), b1 = __shfl(b, lane + 18), a2 = __shfl(a, lane + 36), b2 = __shfl(b, lane + 36);
        if (lane < 18) {
          float gt = rbf(a + a1 + a2);
          const float up = rbf(b + b1 + b2);
          gt = gt > 0.f ? gt : 0.f;
          lds[L_HV + lane] = rbf(gt * up);
        }
      }
      const float m = __uint_as_float(gm[0]);
      float M = m;
      M = fmaxf(M, dpp<ROR4>(M)); M = fmaxf(M, dpp<XOR8>(M)); M = sw16_max(M); M = sw32_max(M);
      const float wt = __expf(m - M);
      float v[5] = {wt * __uint_as_float(gm[1]), wt * __uint_as_float(g0[0]), wt * __uint_as_float(g0[1]), wt * __uint_as_float(g1[0]), wt * __uint_as_float(g1[1])};
#pragma unroll
      for (int j = 0; j < 5; ++j) { v[j] += dpp<ROR4>(v[j]); v[j] += dpp<XOR8>(v[j]); v[j] = sw16_sum(v[j], v[j]); v[j] = sw32_sum(v[j], v[j]); }
      if (lane < 4) {
        const float inv = 1.0f / v[0];
        f32x4 o4 = {rbf(v[1] * inv), rbf(v[2] * inv), rbf(v[3] * inv), rbf(v[4] * inv)};
        *reinterpret_cast<f32x4*>(&lds[L_OV + 4 * lane]) = o4;
      }
    }
    BAR();
    return;
  }

  const size_t kvbase = ((size_t)h * SK + (SK - W) + 256 * s + 32 * w + r4) * DH + 8 * c16;
  const v4u xa = *reinterpret_cast<const v4u*>(p.x + 8 * lane);
  const v4u xb = *reinterpret_cast<const v4u*>(p.x + 512 + 8 * (lane & 7));
  const int gurow0 = 192 * (hu / 72) + 24 * w;
  const unsigned xq0 = p.x[qrow0 + rs8], xq1 = p.x[qrow0 + 8 + rs8];
  const unsigned xg0 = p.x[gurow0 + rs8], xg1 = p.x[gurow0 + 8 + rs8], xg2 = p.x[gurow0 + 16 + rs8];
  const v4u wq0 = ldw(wqp), wq1 = ldw(wqp + (size_t)8 * NQ);
  const unsigned e0 = *reinterpret_cast<const unsigned*>(p.ws + WS_E);
  const unsigned xfin = p.x[16 * (li < 0 ? 0 : (li > 35 ? 35 : li)) + (tid & 15)];
  PIN();
  BAR();
  PIN();
  v4u kr[8], vr[8], gr[3], ur[3], wor[3], wdr[3];
  const size_t guo = heavy ? (size_t)(gurow0 + rs8) * F + 64 * (hu % 72) + 8 * ch8 : (size_t)0;
  const size_t gust = heavy ? (size_t)8 * F : (size_t)0;
  const int tq = tid < 504 ? tid : 503;
  const int ch72 = tq % 72, rg7 = tq / 72;
#define LD_K0() _Pragma("unroll") for (int i = 0; i < 8; ++i) kr[i] = ldw(p.kc + kvbase + (size_t)(4 * i) * DH);
#define LD_GU0() _Pragma("unroll") for (int i = 0; i < 3; ++i) { gr[i] = ldw(p.wg + guo + gust * i); ur[i] = ldw(p.wu + guo + gust * i); }
#define LD_V() _Pragma("unroll") for (int i = 0; i < 8; ++i) { vr[i] = ldw(p.vc + kvbase + (size_t)(4 * i) * DH); }
#define LD_DO() _Pragma("unroll") for (int i = 0; i < 3; ++i) { const int r = min(rg7 + 7 * i, 17); wdr[i] = ldw(p.wd + (size_t)(18 * c + r) * D + 8 * ch72); } \
                _Pragma("unroll") for (int i = 0; i < 3; ++i) { const int r = min(rg7 + 7 * i, 15); wor[i] = ldw(p.wo + (size_t)(16 * c + r) * D + 8 * ch72); }
  LD_GU0();
  PIN();
  const unsigned tag0 = e0 + 1u, tag1 = ~tag0;
  const LnS lns = ln_stats(lane, xa, xb);
  q_part(lds, lane, w, lns, xq0, xq1, wq0, wq1);
  BAR();
  LD_K0();
  PIN();
  auto gu_partial = [&]() {
  {
    float ag[8] = {0, 0, 0, 0, 0, 0, 0, 0}, au[8] = {0, 0, 0, 0, 0, 0, 0, 0};
    const float xv[3] = {ln_apply(__uint_as_float(xg0 << 16), lns), ln_apply(__uint_as_float(xg1 << 16), lns), ln_apply(__uint_as_float(xg2 << 16), lns)};
#pragma unroll
    for (int i = 0; i < 3; ++i) {
      fma8(ag, xv[i], gr[i]);
      fma8(au, xv[i], ur[i]);
    }
    tr32<4>(ag); tr16<2>(ag); trd<1, XOR8, 8>(ag, lane);
    tr32<4>(au); tr16<2>(au); trd<1, XOR8, 8>(au, lane);
    lds[L_GUP + 128 * w + 8 * ch8 + rs8] = ag[0];
    lds[L_GUP + 128 * w + 64 + 8 * ch8 + rs8] = au[0];
    asm volatile("s_waitcnt lgkmcnt(0)" ::: "memory");
    unsigned old = 0;
    if (lane == 0) old = __hip_atomic_fetch_add(reinterpret_cast<unsigned*>(lds) + L_CNT, 1u, __ATOMIC_RELAXED, __HIP_MEMORY_SCOPE_WORKGROUP);
    old = __builtin_amdgcn_readfirstlane(old);
    if (old == 7u && heavy) {
      asm volatile("" ::: "memory");
      float a = 0.f, b = 0.f;
#pragma unroll
      for (int ww = 0; ww < 8; ++ww) { a += lds[L_GUP + 128 * ww + lane]; b += lds[L_GUP + 128 * ww + 64 + lane]; }
      const v4u g = {__float_as_uint(a), __float_as_uint(b), tag0, tag1};
      __builtin_amdgcn_raw_buffer_store_b128(g, rs, WS_HG + hu * 1024 + lane * 16, 0, 16);
    }
  }
  };
  gu_partial();
  LD_V();
  PIN();
  while (__hip_atomic_load(reinterpret_cast<unsigned*>(lds) + L_CNT + 1, __ATOMIC_RELAXED, __HIP_MEMORY_SCOPE_WORKGROUP) == 0u) __builtin_amdgcn_s_sleep(1);
  asm volatile("" ::: "memory");
  PIN();
  float pv[16];
  {
    v4u qv[2];
#pragma unroll
    for (int g = 0; g < 2; ++g) qv[g] = *reinterpret_cast<const v4u*>(&lds[L_QB + 64 * g + 4 * c16]);
    float x[16];
#pragma unroll
    for (int i = 0; i < 8; ++i)
#pragma unroll
      for (int g = 0; g < 2; ++g) {
        float d = 0.f;
#pragma unroll
        for (int k = 0; k < 4; ++k) d = dot2(kr[i][k], qv[g][k], d);
        x[2 * i + g] = d;
      }
    trd<8, XOR8, 8>(x, lane); trd<4, XOR7, 4>(x, lane); trd<2, XOR2, 2>(x, lane); trd<1, XOR1, 1>(x, lane);
    const float sc = rbf(rbf(x[0]) * 0.08838834764831845f);
    float m = sc;
    m = fmaxf(m, dpp<XOR2>(m)); m = fmaxf(m, dpp<ROR4>(m)); m = fmaxf(m, dpp<XOR8>(m)); m = sw16_max(m); m = sw32_max(m);
    const float pe = __expf(sc - m);
    float l = pe;
    l += dpp<XOR2>(l); l += dpp<ROR4>(l); l += dpp<XOR8>(l); l = sw16_sum(l, l); l = sw32_sum(l, l);
    if (lane < 2) { lds[L_MRG + (w * 2 + lane) * 132 + 128] = m; lds[L_MRG + (w * 2 + lane) * 132 + 129] = l; }
    lds[L_PEX + 64 * w + lane] = pe;
    asm volatile("s_waitcnt lgkmcnt(0)" ::: "memory");
#pragma unroll
    for (int u = 0; u < 4; ++u) {
      const f32x4 t = *reinterpret_cast<const f32x4*>(&lds[L_PEX + 64 * w + 16 * r4 + 4 * u]);
      pv[4 * u] = t[0]; pv[4 * u + 1] = t[1]; pv[4 * u + 2] = t[2]; pv[4 * u + 3] = t[3];
    }
  }
  LD_DO();
  PIN();
  {
    float acc[16];
#pragma unroll
    for (int k = 0; k < 16; ++k) acc[k] = 0.f;
#pragma unroll
    for (int i = 0; i < 8; ++i) {
      fma8(acc, pv[2 * i], vr[i]);
      fma8(acc + 8, pv[2 * i + 1], vr[i]);
    }
    tr32<8>(acc); tr16<4>(acc);
    const f32x4 o4 = {acc[0], acc[1], acc[2], acc[3]};
    *reinterpret_cast<f32x4*>(&lds[L_MRG + (w * 2 + (lane >> 5)) * 132 + 8 * c16 + 4 * ((lane >> 4) & 1)]) = o4;
  }
  BAR();
  BAR();
  if (tid < 504) {
    float acc[8] = {0, 0, 0, 0, 0, 0, 0, 0};
#pragma unroll
    for (int i = 0; i < 3; ++i) {
      const int r = rg7 + 7 * i;
      const float hv = r < 18 ? lds[L_HV + min(r, 17)] : 0.f, ov = r < 16 ? lds[L_OV + min(r, 15)] : 0.f;
      fma8(acc, hv, wdr[i]);
      fma8(acc, ov, wor[i]);
    }
    const f32x4 a0 = {acc[0], acc[1], acc[2], acc[3]}, a1 = {acc[4], acc[5], acc[6], acc[7]};
    *reinterpret_cast<f32x4*>(&lds[L_FIN + 576 * rg7 + 8 * ch72]) = a0;
    *reinterpret_cast<f32x4*>(&lds[L_FIN + 576 * rg7 + 8 * ch72 + 4]) = a1;
  }
  BAR();
  if (tid < 288) {
    float a = 0.f, b = 0.f;
#pragma unroll
    for (int r = 0; r < 7; ++r) { const f32x2 v = *reinterpret_cast<const f32x2*>(&lds[L_FIN + 576 * r + 2 * tid]); a += v[0]; b += v[1]; }
    const v4u g = {__float_as_uint(a), __float_as_uint(b), tag0, tag1};
    __builtin_amdgcn_raw_buffer_store_b128(g, rs, WS_FG + c * 4608 + tid * 16, 0, 16);
  }
  if (heavy || li >= 36) return;
  {
    const int gi = tid & 7, p0 = tid >> 3;
    v4u g[4];
    for (unsigned spin = 0;; ++spin) {
      bool ok = true;
#pragma unroll
      for (int k = 0; k < 4; ++k) g[k] = __builtin_amdgcn_raw_buffer_load_b128(rs, WS_FG + (p0 + 64 * k) * 4608 + li * 128 + gi * 16, 0, 16);
#pragma unroll
      for (int k = 0; k < 4; ++k) ok &= (g[k][2] == tag0) & (g[k][3] == tag1);
      if (__all(ok) || spin > SPIN_CAP) break;
      asm volatile("" ::: "memory");
    }
    float a = 0.f, b = 0.f;
#pragma unroll
    for (int k = 0; k < 4; ++k) { a += __uint_as_float(g[k][0]); b += __uint_as_float(g[k][1]); }
#pragma unroll
    for (int k = 0; k < 1; ++k) { a += dpp<XOR8>(a); b += dpp<XOR8>(b); a = sw16_sum(a, a); b = sw16_sum(b, b); a = sw32_sum(a, a); b = sw32_sum(b, b); }
    if (lane < 8) { lds[L_RED + 16 * w + 2 * lane] = a; lds[L_RED + 16 * w + 2 * lane + 1] = b; }
    BAR();
    if (tid < 16) {
      float t = 0.f;
#pragma unroll
      for (int ww = 0; ww < 8; ++ww) t += lds[L_RED + 16 * ww + tid];
      const int n = 16 * li + tid;
      const float xv = __uint_as_float(xfin << 16);
      p.out[n] = (u16)f2bf(xv + t);
    }
    if (li == 0 && tid == 0) *reinterpret_cast<unsigned*>(p.ws + WS_E) = tag0;
  }
}

extern "C" void kernel_launch(void* const* d_in, const int* in_sizes, int n_in, void* d_out, int out_size, void* d_ws, size_t ws_size,
                              hipStream_t stream) {
  Params p;
  p.x = (const u16*)d_in[0];
  p.kc = (const u16*)d_in[1];
  p.vc = (const u16*)d_in[2];
  p.wq = (const u16*)d_in[3];
  p.wo = (const u16*)d_in[4];
  p.wg = (const u16*)d_in[6];
  p.wu = (const u16*)d_in[7];
  p.wd = (const u16*)d_in[8];
  p.out = (u16*)d_out;
  p.ws = (unsigned char*)d_ws;
  mega_kernel<<<256, NTHR, 0, stream>>>(p);
}
```
